```python
import torch

def _softmax(x, axis=-1):
    return torch.softmax(x, dim=axis)
def _gelu(x):
    return torch.nn.functional.gelu(x, approximate="tanh")
def _silu(x):
    return torch.nn.functional.silu(x)
def _sigmoid(x):
    return torch.sigmoid(x)
def _layernorm(x, axis=-1, eps=1e-5):
    m = x.mean(dim=axis, keepdim=True)
    v = x.var(dim=axis, keepdim=True, unbiased=False)
    return (x - m) / torch.sqrt(v + eps)
def _scatter_add(tgt, idx, src, axis=0):
    return tgt.clone().index_add_(axis, idx, src)
def _segment_sum(src, seg, n):
    out = torch.zeros((n,) + src.shape[1:], dtype=src.dtype)
    return out.index_add_(0, seg, src)
def _pad(x, pw):
    flat = tuple(v for pair in reversed(pw) for v in pair)
    return torch.nn.functional.pad(x, flat)

kernel_name = 'sequential_preLN__softmax_full_mha__mixer_s_mlp__prompt'

def setup_inputs():

    _w = (lambda a: (lambda t: t.to(torch.bfloat16) if t.is_floating_point() else t)(torch.as_tensor(a)))
    def _A_setup():
        B, S, D = 1, 2048, 640
        return {
            "x": _w((lambda *s: torch.randn(*s, dtype=torch.bfloat16) * 0.05)(B, S, D)),
            "wqkv": _w((lambda *s: torch.randn(*s, dtype=torch.bfloat16) * 0.05)(D, 3 * D)),
            "wo": _w((lambda *s: torch.randn(*s, dtype=torch.bfloat16) * 0.05)(D, D)),
        }
    _A_in = _A_setup()

    def _B_setup():
        B, S, D, F = 1, 2048, 640, 1536
        return {
            "x":  _w((lambda *s: torch.randn(*s, dtype=torch.bfloat16) * 0.05)(B, S, D)),
            "w1": _w((lambda *s: torch.randn(*s, dtype=torch.bfloat16) * 3.2)(D, D) * (D ** -0.5)),
            "w2": _w((lambda *s: torch.randn(*s, dtype=torch.bfloat16) * 3.2)(D, D) * (D ** -0.5)),
            "w3": _w((lambda *s: torch.randn(*s, dtype=torch.bfloat16) * 3.2)(D, F) * (D ** -0.5)),
            "w4": _w((lambda *s: torch.randn(*s, dtype=torch.bfloat16) * 3.2)(F, D) * (F ** -0.5)),
        }
    _B_in = _B_setup()

    return {
        "A_x": _A_in["x"],
        "A_wqkv": _A_in["wqkv"],
        "A_wo": _A_in["wo"],
        "B_x": _B_in["x"],
        "B_w1": _B_in["w1"],
        "B_w2": _B_in["w2"],
        "B_w3": _B_in["w3"],
        "B_w4": _B_in["w4"],
    }

def reference(A_x, A_wqkv, A_wo, B_x, B_w1, B_w2, B_w3, B_w4):
    def _A_ref(x, wqkv, wo):
        B, S, D = x.shape
        H = 32; Dh = D // H
        qkv = (lambda x, *d: x.permute(*d))((x @ wqkv).reshape(B, S, 3, H, Dh), 2, 0, 3, 1, 4)
        q, k, v = qkv[0], qkv[1], qkv[2]
        scale = Dh ** -0.5
        scores = torch.einsum("bhqd,bhkd->bhqk", q, k) * scale
        p = _softmax(scores, axis=-1)
        o = torch.einsum("bhqk,bhkd->bhqd", p, v)
        return (lambda x, *d: x.permute(*d))(o, 0, 2, 1, 3).reshape(B, S, D) @ wo

    def _B_ref(x, w1, w2, w3, w4):
        B, S, D = x.shape
        h = _gelu(_layernorm(x, axis=-1) @ w1)
        c = D // 4
        g0 = _pad(h[:, :, 0*c:1*c], ((0, 0), (1, 0), (0, 0)))[:, :S, :]
        g1 = _pad(h[:, :, 1*c:2*c], ((0, 0), (0, 1), (0, 0)))[:, 1:, :]
        g2 = _pad(h[:, :, 2*c:3*c], ((0, 0), (2, 0), (0, 0)))[:, :S, :]
        g3 = _pad(h[:, :, 3*c:   ], ((0, 0), (0, 2), (0, 0)))[:, 2:, :]
        hs = torch.concatenate([g0, g1, g2, g3], axis=-1) @ w2
        x1 = x + hs
        h2 = _gelu(_layernorm(x1, axis=-1) @ w3) @ w4
        return x1 + h2

    x = A_x
    h = x + _A_ref(_layernorm(x, axis=-1), A_wqkv, A_wo)
    h2 = h + _B_ref(_layernorm(h, axis=-1), B_w1, B_w2, B_w3, B_w4)
    return h2
```

```cpp
#include <hip/hip_runtime.h>
#include <hip/hip_bf16.h>
#include <stdint.h>
#include <type_traits>

typedef __attribute__((ext_vector_type(8))) short bf16x8;
typedef __attribute__((ext_vector_type(4))) short bf16x4;
typedef __attribute__((ext_vector_type(16))) float f32x16;
typedef __attribute__((ext_vector_type(4))) float f32x4;
typedef __attribute__((ext_vector_type(2))) float f32x2;
typedef __attribute__((ext_vector_type(4))) unsigned u32x4;
typedef __attribute__((ext_vector_type(2))) unsigned u32x2;
typedef __attribute__((ext_vector_type(2))) __bf16 bfv2;
#define LAS __attribute__((address_space(3)))
#define DI __device__ __forceinline__

constexpr int S_ = 2048, D_ = 640, H_ = 32, F_ = 1536;
constexpr float EPS = 1e-5f;
constexpr float QSCALE = 0.22360679774997896f * 1.4426950408889634f;

constexpr size_t SZ_ACT = (size_t)S_ * D_ * 2;
constexpr int KVT = 2624;
constexpr size_t WS_Q = 0;
constexpr size_t WS_KV = WS_Q + SZ_ACT;
constexpr size_t WS_O = WS_KV + (size_t)H_ * 64 * KVT;
constexpr size_t WS_H = WS_O + SZ_ACT;
constexpr size_t WS_HS = WS_H + SZ_ACT;
constexpr size_t WS_X1 = WS_HS + SZ_ACT;
constexpr size_t WS_G = WS_X1 + SZ_ACT;
constexpr size_t WS_STH = WS_G + (size_t)S_ * F_ * 2;
constexpr size_t WS_STX = WS_STH + (size_t)S_ * 64;
constexpr size_t WS_WO = WS_STX + (size_t)S_ * 64;
constexpr size_t WS_W1 = WS_WO + (size_t)D_ * D_ * 2;
constexpr size_t WS_W2 = WS_W1 + (size_t)D_ * D_ * 2;
constexpr size_t WS_W3 = WS_W2 + (size_t)D_ * D_ * 2;
constexpr size_t WS_W4 = WS_W3 + (size_t)D_ * F_ * 2;
constexpr size_t WS_END = WS_W4 + (size_t)D_ * F_ * 2;

DI unsigned pk2(float a, float b) { bfv2 v = {(__bf16)a, (__bf16)b}; return __builtin_bit_cast(unsigned, v); }
DI float blo(unsigned u) { return __uint_as_float(u << 16); }
DI float bhi(unsigned u) { return __uint_as_float(u & 0xffff0000u); }
DI int rfl(int x) { return __builtin_amdgcn_readfirstlane(x); }
DI float xsum32(float v) { auto r = __builtin_amdgcn_permlane32_swap(__float_as_uint(v), __float_as_uint(v), false, false); return __uint_as_float(r[0]) + __uint_as_float(r[1]); }
DI float xsum16(float v) { auto r = __builtin_amdgcn_permlane16_swap(__float_as_uint(v), __float_as_uint(v), false, false); return __uint_as_float(r[0]) + __uint_as_float(r[1]); }
DI float xmax32(float v) { auto r = __builtin_amdgcn_permlane32_swap(__float_as_uint(v), __float_as_uint(v), false, false); return fmaxf(__uint_as_float(r[0]), __uint_as_float(r[1])); }
DI float gelu_t(float x) {
    constexpr float c1 = -2.f * 0.7978845608028654f * 1.4426950408889634f, c2 = c1 * 0.044715f;
    const float e = __builtin_amdgcn_exp2f(x * __builtin_fmaf(x * x, c2, c1));
    return x * __builtin_amdgcn_rcpf(1.f + e);
}
template <int OFF> DI void glds(const void* sbase, unsigned voff, unsigned m0v) {
    asm volatile("s_mov_b32 m0, %2\n\ts_nop 0\n\tglobal_load_lds_dwordx4 %0, %1 offset:%3" ::"v"(voff), "s"(sbase), "s"(m0v), "i"(OFF) : "memory");
}
template <int OFF> DI void glds_same(const void* sbase, unsigned voff) {
    asm volatile("global_load_lds_dwordx4 %0, %1 offset:%2" ::"v"(voff), "s"(sbase), "i"(OFF) : "memory");
}
template <int OFF> DI u32x4 gld16(const void* sbase, unsigned voff) { u32x4 r; asm volatile("global_load_dwordx4 %0, %1, %2 offset:%3" : "=v"(r) : "v"(voff), "s"(sbase), "i"(OFF) : "memory"); return r; }
template <int OFF> DI u32x2 gld8(const void* sbase, unsigned voff) { u32x2 r; asm volatile("global_load_dwordx2 %0, %1, %2 offset:%3" : "=v"(r) : "v"(voff), "s"(sbase), "i"(OFF) : "memory"); return r; }
template <int N> DI void vmwait() { asm volatile("s_waitcnt vmcnt(%0)" ::"i"(N) : "memory"); }
DI void bar() { asm volatile("s_barrier" ::: "memory"); }
DI void lgbar() { asm volatile("s_waitcnt lgkmcnt(0)\n\ts_barrier" ::: "memory"); }
#define PIN(x) asm volatile("" : "+v"(x))
DI bf16x8 lds16(const LAS char* p) { return *(const LAS bf16x8*)p; }
DI bf16x4 lds8(const LAS char* p) { return *(const LAS bf16x4*)p; }
DI bf16x4 ldstr(const LAS char* p) { return __builtin_amdgcn_ds_read_tr16_b64_v4i16((LAS bf16x4*)p); }
#define MFMA32(a, b, c) __builtin_amdgcn_mfma_f32_32x32x16_bf16((a), (b), (c), 0, 0, 0)
#define MFMA32K8(a, b, c) __builtin_amdgcn_mfma_f32_32x32x8bf16_1k((a), (b), (c), 0, 0, 0)
#define MFMA16(a, b, c) __builtin_amdgcn_mfma_f32_16x16x32_bf16((a), (b), (c), 0, 0, 0)

template <bool VSEC>
DI void qkv_body(const char* __restrict__ x, const char* __restrict__ w, char* __restrict__ ws, LAS char* lds, int mp, int nt) {
    const int tid = threadIdx.x, lane = tid & 63, wv = rfl(tid >> 6), wm = wv >> 1, wn = wv & 1;
    const int r32 = lane & 31, h = lane >> 5;
    const unsigned lds0 = (unsigned)(uintptr_t)lds;
    const unsigned xo0 = (lane >> 3) * 1280 + (((lane & 7) ^ (lane >> 4)) << 4), xo1 = xo0 ^ 64;
    const unsigned wo_ = (lane >> 4) * 3840 + (((lane & 15) ^ ((lane >> 4) << 2)) << 4);
    const char* xs = x + (size_t)(mp * 128 + wv * 32) * 1280;
    const char* wsrc = w + (size_t)(wv * 16) * 3840 + nt * 256;
    auto stage = [&](int kc) {
        const unsigned sl = lds0 + (kc % 5) * 32768 + wv * 4096;
        const char* xa = xs + kc * 128;
        const char* wa = wsrc + (size_t)kc * 64 * 3840;
#pragma unroll
        for (int i = 0; i < 4; ++i) {
            glds<0>(xa + i * 10240, (i & 1) ? xo1 : xo0, sl + i * 1024);
            glds<0>(wa + i * 15360, wo_, sl + 16384 + i * 1024);
        }
    };
#pragma unroll
    for (int c = 0; c < 5; ++c) stage(c);
    unsigned xa[4][2], wa[2];
#pragma unroll
    for (int mi = 0; mi < 2; ++mi) {
        const int row = wm * 64 + ((mi ^ wn) << 5) + r32;
#pragma unroll
        for (int kk = 0; kk < 4; ++kk) xa[kk][mi] = row * 128 + ((((kk << 1) + h) ^ ((row >> 1) & 7)) << 4);
    }
#pragma unroll
    for (int ni = 0; ni < 2; ++ni) {
        const int q = (lane & 15) >> 2, p = lane & 3, gi = (lane >> 4) & 1;
        wa[ni] = 16384 + (8 * h + q) * 256 + ((((wn << 1) + (ni ^ wm)) ^ q) << 6) + gi * 32 + p * 8;
    }
    bf16x8 sel;
    {
        const short one = ((lane & 15) == ((lane >> 4) & 1)) ? (short)0x3F80 : (short)0;
#pragma unroll
        for (int j = 0; j < 8; ++j) sel[j] = one;
    }
    f32x16 acc[2][2] = {};
    f32x4 cs = {};
    float sx = 0.f, sxx = 0.f;
#pragma unroll
    for (int c = 0; c < 10; ++c) {
        if (c == 0) vmwait<32>(); else if (c <= 6) vmwait<24>(); else if (c == 7) vmwait<16>(); else if (c == 8) vmwait<8>(); else vmwait<0>();
        lgbar();
        if (c >= 1 && c + 4 < 10) stage(c + 4);
        const LAS char* sl = lds + (c % 5) * 32768;
#pragma unroll
        for (int kk = 0; kk < 4; ++kk) {
            bf16x8 xf[2], wf[2];
#pragma unroll
            for (int mi = 0; mi < 2; ++mi) xf[mi] = lds16(sl + xa[kk][mi]);
#pragma unroll
            for (int ni = 0; ni < 2; ++ni) {
                const bf16x4 a = ldstr(sl + wa[ni] + kk * 4096), b = ldstr(sl + wa[ni] + kk * 4096 + 1024);
                wf[ni] = (bf16x8){a[0], a[1], a[2], a[3], b[0], b[1], b[2], b[3]};
            }
#pragma unroll
            for (int ni = 0; ni < 2; ++ni)
#pragma unroll
                for (int mi = 0; mi < 2; ++mi)
                    acc[ni][mi] = VSEC ? MFMA32(xf[mi], wf[ni], acc[ni][mi]) : MFMA32(wf[ni], xf[mi], acc[ni][mi]);
            cs = MFMA16(sel, wf[0], cs);
            const u32x4 xu = __builtin_bit_cast(u32x4, xf[0]);
#pragma unroll
            for (int j = 0; j < 4; ++j) {
                const float a = blo(xu[j]), b = bhi(xu[j]);
                sx += a; sxx = __builtin_fmaf(a, a, sxx); sx += b; sxx = __builtin_fmaf(b, b, sxx);
            }
        }
    }
    LAS float* st_mu = (LAS float*)lds; LAS float* st_rs = st_mu + 128; LAS float* st_cs = st_mu + 256;
    {
        sx = xsum32(sx); sxx = xsum32(sxx);
        const float mu = sx * (1.f / D_), var = fmaxf(sxx * (1.f / D_) - mu * mu, 0.f);
        float rs = __builtin_amdgcn_rsqf(var + EPS);
        if (nt < 5) rs *= QSCALE;
        if (h == 0) { st_mu[wm * 64 + wn * 32 + r32] = mu; st_rs[wm * 64 + wn * 32 + r32] = rs; }
        if (lane < 16) { st_cs[wn * 64 + wm * 32 + lane] = cs[0]; st_cs[wn * 64 + wm * 32 + 16 + lane] = cs[1]; }
    }
    lgbar();
    if (!VSEC) {
        const bool isq = nt < 5;
        const int nsec = (isq ? nt : nt - 5) * 128;
#pragma unroll
        for (int mi = 0; mi < 2; ++mi) {
            const int rl = wm * 64 + ((mi ^ wn) << 5) + r32, m = mp * 128 + rl;
            const float mu = st_mu[rl], rs = st_rs[rl];
#pragma unroll
            for (int ni = 0; ni < 2; ++ni) {
                const int cl0 = wn * 64 + ((ni ^ wm) << 5);
#pragma unroll
                for (int q = 0; q < 4; ++q) {
                    const int cl = cl0 + 8 * q + 4 * h;
                    const f32x4 c4 = *(const LAS f32x4*)(st_cs + cl);
                    u32x2 o;
                    o[0] = pk2((acc[ni][mi][4 * q] - mu * c4[0]) * rs, (acc[ni][mi][4 * q + 1] - mu * c4[1]) * rs);
                    o[1] = pk2((acc[ni][mi][4 * q + 2] - mu * c4[2]) * rs, (acc[ni][mi][4 * q + 3] - mu * c4[3]) * rs);
                    const int qg = (nsec + cl) >> 2, head = qg / 5, qi = qg - head * 5;
                    unsigned off;
                    if (isq) off = (unsigned)WS_Q + (head * S_ + m) * 40 + qi * 8;
                    else off = (unsigned)WS_KV + (head * 64 + (m >> 5)) * KVT + (qi < 4 ? (qi >> 1) * 512 + r32 * 16 + (qi & 1) * 8 : 1024 + r32 * 8);
                    *(u32x2*)(ws + off) = o;
                }
            }
        }
    } else {
#pragma unroll
        for (int ni = 0; ni < 2; ++ni) {
            const int cl = wn * 64 + ((ni ^ wm) << 5) + r32, n = (nt - 10) * 128 + cl, head = n / 20, d = n - head * 20;
            const float cn = st_cs[cl];
#pragma unroll
            for (int mi = 0; mi < 2; ++mi) {
                const int rl0 = wm * 64 + ((mi ^ wn) << 5);
                char* dst = ws + WS_KV + (size_t)(head * 64 + mp * 4 + (rl0 >> 5)) * KVT + 1280 + h * 336;
#pragma unroll
                for (int s = 0; s < 2; ++s) {
                    u32x4 o;
#pragma unroll
                    for (int q = 0; q < 2; ++q) {
                        const int rl = rl0 + 16 * s + 8 * q + 4 * h, r = 8 * s + 4 * q;
                        const f32x4 mu = *(const LAS f32x4*)(st_mu + rl), rs = *(const LAS f32x4*)(st_rs + rl);
                        o[2 * q] = pk2((acc[ni][mi][r] - mu[0] * cn) * rs[0], (acc[ni][mi][r + 1] - mu[1] * cn) * rs[1]);
                        o[2 * q + 1] = pk2((acc[ni][mi][r + 2] - mu[2] * cn) * rs[2], (acc[ni][mi][r + 3] - mu[3] * cn) * rs[3]);
                    }
                    *(u32x4*)(dst + s * 672 + d * 16) = o;
                    if (d == 0) *(u32x4*)(dst + s * 672 + 320) = (u32x4){0x3F803F80u, 0x3F803F80u, 0x3F803F80u, 0x3F803F80u};
                }
            }
        }
    }
}

__global__ void __launch_bounds__(256, 1) k_qkv(const char* __restrict__ x, const char* __restrict__ w, char* __restrict__ ws) {
    extern __shared__ __attribute__((aligned(16))) char smem[];
    const int id = (blockIdx.x & 7) * 30 + (blockIdx.x >> 3);
    const int rg = id / 60, rem = id - rg * 60, nt = rem >> 2, mp = rg * 4 + (rem & 3);
    if (nt >= 10) qkv_body<true>(x, w, ws, (LAS char*)smem, mp, nt);
    else qkv_body<false>(x, w, ws, (LAS char*)smem, mp, nt);
}

constexpr int AT_SLOT = 16 * KVT;
constexpr int AT_RT = 2 * AT_SLOT;
constexpr int AT_FLAG = AT_RT + 5 * 6144;
constexpr int AT_LDS = AT_FLAG + 16;

struct WPtrs { const char* wo; const char* w1; const char* w2; const char* w3; const char* w4; };

DI void at_stage(const char* kv, unsigned lane16, unsigned lds0, int c, int wv) {
    const char* src = kv + c * AT_SLOT + wv * 1024;
    const unsigned dst = lds0 + (c & 1) * AT_SLOT + wv * 1024;
    glds<0>(src, lane16, dst);
    glds<0>(src + 16384, lane16, dst + 16384);
    if (wv < 9) glds<0>(src + 32768, lane16, dst + 32768);
}

__global__ void __launch_bounds__(1024, 4) k_attn(WPtrs wp, char* __restrict__ ws) {
    extern __shared__ __attribute__((aligned(16))) char smem[];
    LAS char* lds = (LAS char*)smem;
    const unsigned lds0 = (unsigned)(uintptr_t)lds;
    const int tid = threadIdx.x, lane = tid & 63, wv = rfl(tid >> 6), r32 = lane & 31, h = lane >> 5;
    const int b = blockIdx.x, head = (b & 7) * 4 + (b >> 6), qb = (b >> 3) & 7, qt = wv & 7, half = wv >> 3;
    const char* kv = ws + WS_KV + (size_t)head * 64 * KVT;
    const unsigned lane16 = lane * 16;
    at_stage(kv, lane16, lds0, 0, wv);
    const int qrow = qb * 256 + qt * 32 + r32;
    bf16x8 q1; bf16x4 q2;
    {
        const char* qp = ws + WS_Q + (size_t)head * S_ * 40;
        const unsigned qo = qrow * 40;
        u32x2 a = gld8<0>(qp, qo + h * 16), c = gld8<8>(qp, qo + h * 16), e = gld8<32>(qp, qo);
        if (tid == 0) *(LAS u32x4*)(lds + AT_FLAG) = (u32x4){0u, 0u, 0u, 0u};
        asm volatile("s_waitcnt vmcnt(0)" : "+v"(a), "+v"(c), "+v"(e)::"memory");
        q1 = __builtin_bit_cast(bf16x8, (u32x4){a[0], a[1], c[0], c[1]});
        if (h) e = (u32x2){0u, 0u};
        q2 = __builtin_bit_cast(bf16x4, e);
    }
    const int unit = wv * 256 + b;
    const bool rt = wv < 5 && unit < 1184;
    int rt_nt = 5, rt_nb = 0; char* rt_dst = nullptr;
    const unsigned ka = lane16 + half * 8 * KVT, kb = r32 * 8 + 1024 + half * 8 * KVT;
    const unsigned va = 1280 + h * 336 + (r32 < 20 ? r32 : (r32 < 28 ? 20 : 16)) * 16 + half * 8 * KVT;
    f32x16 cinit = {};
    PIN(cinit);
    for (int attempt = 0;; ++attempt) {
        f32x16 o = {}, sa, sb;
        vmwait<0>(); lgbar();
        at_stage(kv, lane16, lds0, 1, wv);
        if (attempt == 0 && rt) {
            int mat, ks;
            if (unit < 480) { mat = unit / 160; const int u = unit - mat * 160; rt_nb = u / 20; ks = u - rt_nb * 20; }
            else if (unit < 800) { mat = 3; const int u = unit - 480; rt_nb = u / 20; ks = u - rt_nb * 20; }
            else { mat = 4; const int u = unit - 800; rt_nb = u / 48; ks = u - rt_nb * 48; }
            const char* src = mat == 0 ? wp.wo : mat == 1 ? wp.w1 : mat == 2 ? wp.w2 : mat == 3 ? wp.w3 : wp.w4;
            rt_nt = mat == 3 ? 6 : 5;
            const int pitch = mat == 3 ? 3072 : 1280, lpr = rt_nt * 2;
            rt_dst = ws + (mat == 0 ? WS_WO : mat == 1 ? WS_W1 : mat == 2 ? WS_W2 : mat == 3 ? WS_W3 : WS_W4) + (size_t)(rt_nb * (mat == 4 ? 48 : 20) + ks) * rt_nt * 1024;
            src += rt_nb * rt_nt * 32;
#pragma unroll 1
            for (int i = 0; i < rt_nt; ++i) {
                const int e = i * 64 + lane, rr = e / lpr, cc = e - rr * lpr;
                int k = ks * 32 + rr;
                if (mat == 0) {
                    const int sg = k >> 3, j = k & 7, p = sg / 5, u = sg - p * 5;
                    k = 40 * p + (u == 4 ? 16 + (j >> 2) * 20 : (u >> 1) * 20 + (u & 1) * 4 + (j >> 2) * 8) + (j & 3);
                }
                glds<0>(src, k * pitch + cc * 16, lds0 + AT_RT + wv * 6144 + i * 1024);
            }
        }
        sa = MFMA32(lds16(lds + ka), q1, cinit);
        sa = MFMA32K8(lds8(lds + kb), q2, sa);
#define SBAR() __builtin_amdgcn_sched_barrier(0)
#define EXP4(A, B, S, I) unsigned A = pk2(__builtin_amdgcn_exp2f(S[I]), __builtin_amdgcn_exp2f(S[I + 1])), \
                                  B = pk2(__builtin_amdgcn_exp2f(S[I + 2]), __builtin_amdgcn_exp2f(S[I + 3])); asm volatile("" : "+v"(A), "+v"(B)); SBAR()
#define TILE(t, SC, SN) do { \
            const bf16x8 v0 = lds16(sl + va + (t) * KVT), v1 = lds16(sl + va + (t) * KVT + 672); \
            if ((t) == 7) { vmwait<0>(); lgbar(); if (c < 2) at_stage(kv, lane16, lds0, c + 2, wv); } \
            const bf16x8 k1 = lds16((t) == 7 ? sn_ + ka : sl + ka + ((t) + 1) * KVT); \
            const bf16x4 k2 = lds8((t) == 7 ? sn_ + kb : sl + kb + ((t) + 1) * KVT); \
            SBAR(); \
            EXP4(w0, w1, SC, 0);  SN = MFMA32(k1, q1, cinit); SBAR(); \
            EXP4(w2, w3, SC, 4);  SN = MFMA32K8(k2, q2, SN); SBAR(); \
            EXP4(w4, w5, SC, 8);  o = MFMA32(v0, __builtin_bit_cast(bf16x8, (u32x4){w0, w1, w2, w3}), o); SBAR(); \
            EXP4(w6, w7, SC, 12); o = MFMA32(v1, __builtin_bit_cast(bf16x8, (u32x4){w4, w5, w6, w7}), o); SBAR(); } while (0)
#pragma unroll 1
        for (int c = 0; c < 4; ++c) {
            const LAS char* sl = lds + (c & 1) * AT_SLOT;
            const LAS char* sn_ = lds + ((c & 1) ^ 1) * AT_SLOT;
            TILE(0, sa, sb); TILE(1, sb, sa); TILE(2, sa, sb); TILE(3, sb, sa);
            TILE(4, sa, sb); TILE(5, sb, sa); TILE(6, sa, sb); TILE(7, sb, sa);
        }
        LAS float* cb = (LAS float*)lds + qt * 12 * 64 + lane;
        if (half) {
#pragma unroll
            for (int r = 0; r < 12; ++r) cb[r * 64] = o[r];
        }
        if (attempt == 0 && rt) {
            const LAS char* st = lds + AT_RT + wv * 6144;
            const int g = lane >> 4, q = (lane & 15) >> 2, p = lane & 3, pitch = rt_nt * 32;
#pragma unroll 1
            for (int t = 0; t < rt_nt; ++t) {
                int col;
                if (rt_nt == 6) col = (t >> 1) * 32 + 8 * p + 4 * (t & 1);
                else if (t == 4) col = ((rt_nb & 1) ? 0 : 64) + 4 * p;
                else col = ((rt_nb & 1) ? 16 : 0) + (t >> 1) * 32 + 8 * p + 4 * (t & 1);
                const LAS char* a = st + (8 * g + q) * pitch + col * 2;
                const bf16x4 lo = ldstr(a), hi = ldstr(a + 4 * pitch);
                *(bf16x8*)(rt_dst + t * 1024 + lane16) = (bf16x8){lo[0], lo[1], lo[2], lo[3], hi[0], hi[1], hi[2], hi[3]};
            }
        }
        lgbar();
        if (!half) {
#pragma unroll
            for (int r = 0; r < 12; ++r) o[r] += cb[r * 64];
            auto lr = __builtin_amdgcn_permlane32_swap(__float_as_uint(o[8]), __float_as_uint(o[8]), false, false);
            const float l = __uint_as_float(lr[1]);
            const bool ok = l >= 0x1p-60f && l <= 0x1p60f;
            if (!__all(ok) && lane == 0) *(LAS unsigned*)(lds + AT_FLAG + attempt * 4) = 1u;
            const float inv = 1.f / l;
            const int sg = 5 * (head >> 1) + 2 * (head & 1) + h, s4 = 5 * (head >> 1) + 4;
            char* ob = ws + WS_O + (size_t)(qrow >> 4) * 20480 + (qrow & 15) * 16;
            *(u32x4*)(ob + sg * 256) = (u32x4){pk2(o[0] * inv, o[1] * inv), pk2(o[2] * inv, o[3] * inv), pk2(o[4] * inv, o[5] * inv), pk2(o[6] * inv, o[7] * inv)};
            if (!h) *(u32x2*)(ob + s4 * 256 + (head & 1) * 8) = (u32x2){pk2(o[8] * inv, o[9] * inv), pk2(o[10] * inv, o[11] * inv)};
        }
        lgbar();
        if (attempt == 1 || rfl(*(const LAS unsigned*)(lds + AT_FLAG)) == 0) break;
        float mx = -INFINITY;
        at_stage(kv, lane16, lds0, 0, wv);
#pragma unroll 1
        for (int c = 0; c < 4; ++c) {
            vmwait<0>(); lgbar();
            if (c < 3) at_stage(kv, lane16, lds0, c + 1, wv);
            const LAS char* sl = lds + (c & 1) * AT_SLOT;
#pragma unroll 1
            for (int t = 0; t < 8; ++t) {
                f32x16 s = MFMA32(lds16(sl + ka + t * KVT), q1, (f32x16){});
                s = MFMA32K8(lds8(sl + kb + t * KVT), q2, s);
#pragma unroll
                for (int r = 0; r < 16; ++r) mx = fmaxf(mx, s[r]);
            }
        }
        mx = xmax32(mx);
        LAS float* mb = (LAS float*)lds + qt * 64 + r32;
        if (!h) mb[half * 32] = mx;
        lgbar();
        mx = fmaxf(mb[0], mb[32]);
#pragma unroll
        for (int r = 0; r < 16; ++r) cinit[r] = -mx;
        lgbar();
        at_stage(kv, lane16, lds0, 0, wv);
    }
}

enum { E_WO = 0, E_W1 = 1, E_W2 = 2, E_W3 = 3, E_W4 = 4 };

DI void row_stats(u32x4 st, bool twice, float& mu, float& rs) {
    float a = __uint_as_float(st[0]) + __uint_as_float(st[2]), q = __uint_as_float(st[1]) + __uint_as_float(st[3]);
    a = xsum32(xsum16(a)); q = xsum32(xsum16(q));
    mu = a * (1.f / D_);
    const float var = fmaxf(q * (1.f / D_) - mu * mu, 0.f);
    rs = __builtin_amdgcn_rsqf(var + EPS);
    if (twice) rs *= __builtin_amdgcn_rsqf(var * rs * rs + EPS);
}

template <int E>
__global__ void __launch_bounds__(256, 1) k_chain(const char* __restrict__ x, char* __restrict__ ws, char* __restrict__ out) {
    constexpr int MW = E == E_W3 ? 2 : 1, NT = E == E_W3 ? 6 : 5, KS = E == E_W4 ? 48 : 20, NC = KS / 4;
    constexpr int RING = E == E_W4 ? 7 : 5;
    constexpr bool NORM = E == E_W1 || E == E_W3;
    constexpr size_t OA = E == E_WO ? WS_O : E == E_W1 ? WS_H : E == E_W2 ? WS_HS : E == E_W3 ? WS_X1 : WS_G;
    constexpr size_t OW = E == E_WO ? WS_WO : E == E_W1 ? WS_W1 : E == E_W2 ? WS_W2 : E == E_W3 ? WS_W3 : WS_W4;
    constexpr int PER = 4 * MW + NT;
    extern __shared__ __attribute__((aligned(16))) char smem[];
    LAS char* lds = (LAS char*)smem;
    const unsigned lds0 = (unsigned)(uintptr_t)lds;
    const int tid = threadIdx.x, lane = tid & 63, wv = rfl(tid >> 6), r = lane & 15, g = lane >> 4;
    const int b = blockIdx.x, i = b >> 3;
    const int nb = MW == 2 ? (i & 15) : (i & 7);
    const int mt0 = (b & 7) * 16 + (MW == 2 ? (i >> 4) * 8 + wv * 2 : (i >> 3) * 4 + wv);
    const unsigned lane16 = lane * 16;
    const int gb = NT == 6 ? nb * 96 : nb * 80 + ((nb & 1) ? 16 : 0);
    const int hb = nb * 80 + ((nb & 1) ? 0 : 64);
    const unsigned hoff = ((hb >> 5) * 64 + (((hb & 31) >> 3) + (g >> 1)) * 16 + r) * 16 + (g & 1) * 8;
    u32x4 st[MW], ra[2], rb[2]; u32x2 ra4, rb4;
    if (E == E_W1 || E == E_W2 || E == E_W3) {
#pragma unroll
        for (int mi = 0; mi < MW; ++mi) st[mi] = gld16<0>(ws + (E == E_W3 ? WS_STX : WS_STH) + (size_t)(mt0 + mi) * 1024, r * 64 + g * 16);
    }
    if (E == E_WO) {
        const char* xb = x + (size_t)mt0 * 16 * 1280;
        ra[0] = gld16<0>(xb + gb * 2, r * 1280 + g * 16); ra[1] = gld16<64>(xb + gb * 2, r * 1280 + g * 16); ra4 = gld8<0>(xb + hb * 2, r * 1280 + g * 8);
    }
    if (E == E_W2 || E == E_W4) {
        const char* hp = ws + WS_H + (size_t)mt0 * 20480;
        ra[0] = gld16<0>(hp + (gb >> 5) * 1024, lane16); ra[1] = gld16<1024>(hp + (gb >> 5) * 1024, lane16); ra4 = gld8<0>(hp, hoff);
    }
    if (E == E_W4) {
        const char* hp = ws + WS_X1 + (size_t)mt0 * 20480;
        rb[0] = gld16<0>(hp + (gb >> 5) * 1024, lane16); rb[1] = gld16<1024>(hp + (gb >> 5) * 1024, lane16); rb4 = gld8<0>(hp, hoff);
    }
    u32x4 a[MW][KS];
    const char* ap = ws + OA + (size_t)mt0 * KS * 1024;
    const char* wp = ws + OW + (size_t)(nb * KS + wv) * NT * 1024;
    auto issue = [&](auto cc) {
        constexpr int c = decltype(cc)::value;
        const char* wsrc = wp + c * 4 * NT * 1024;
        const unsigned dst = lds0 + ((c % RING) * 4 + wv) * NT * 1024;
        glds<0>(wsrc, lane16, dst); glds_same<1024>(wsrc, lane16); glds_same<2048>(wsrc, lane16); glds_same<3072>(wsrc, lane16);
        glds<0>(wsrc + 4096, lane16, dst + 4096);
        if (NT == 6) glds_same<1024>(wsrc + 4096, lane16);
#pragma unroll
        for (int mi = 0; mi < MW; ++mi) {
            const char* as = ap + (mi * KS + c * 4) * 1024;
            a[mi][c * 4] = gld16<0>(as, lane16); a[mi][c * 4 + 1] = gld16<1024>(as, lane16); a[mi][c * 4 + 2] = gld16<2048>(as, lane16); a[mi][c * 4 + 3] = gld16<3072>(as, lane16);
        }
    };
#define IC(n) std::integral_constant<int, n>{}
    issue(IC(0)); issue(IC(1)); issue(IC(2)); issue(IC(3)); issue(IC(4));
    if constexpr (RING == 7) { issue(IC(5)); issue(IC(6)); }
    f32x4 acc[MW][NT] = {};
    float mu[MW], rs[MW];
    auto step = [&](auto cc) {
        constexpr int c = decltype(cc)::value;
        constexpr int last = (c == 0 ? RING - 1 : (c + RING - 2 < NC - 1 ? c + RING - 2 : NC - 1));
        constexpr int N = (last - c) * PER;
        if constexpr (MW == 2)
            asm volatile("s_waitcnt vmcnt(%8)" : "+v"(a[0][c * 4]), "+v"(a[0][c * 4 + 1]), "+v"(a[0][c * 4 + 2]), "+v"(a[0][c * 4 + 3]),
                         "+v"(a[MW - 1][c * 4]), "+v"(a[MW - 1][c * 4 + 1]), "+v"(a[MW - 1][c * 4 + 2]), "+v"(a[MW - 1][c * 4 + 3]) : "i"(N) : "memory");
        else
            asm volatile("s_waitcnt vmcnt(%4)" : "+v"(a[0][c * 4]), "+v"(a[0][c * 4 + 1]), "+v"(a[0][c * 4 + 2]), "+v"(a[0][c * 4 + 3]) : "i"(N) : "memory");
        lgbar();
        if constexpr (c >= 1 && c + RING - 1 < NC) issue(IC(c + RING - 1 < NC ? c + RING - 1 : 0));
        if constexpr (c == 0) {
            if (E == E_W1 || E == E_W2 || E == E_W3) {
#pragma unroll
                for (int mi = 0; mi < MW; ++mi) { PIN(st[mi]); row_stats(st[mi], E == E_W1, mu[mi], rs[mi]); }
            }
            if (E == E_WO || E == E_W2 || E == E_W4) { PIN(ra[0]); PIN(ra[1]); PIN(ra4); }
            if (E == E_W4) { PIN(rb[0]); PIN(rb[1]); PIN(rb4); }
        }
#pragma unroll
        for (int kk = 0; kk < 4; ++kk) {
            bf16x8 af[MW];
#pragma unroll
            for (int mi = 0; mi < MW; ++mi) {
                u32x4 v = a[mi][c * 4 + kk];
                if (NORM) {
                    const float sh = -mu[mi] * rs[mi];
#pragma unroll
                    for (int j = 0; j < 4; ++j) v[j] = pk2(__builtin_fmaf(blo(v[j]), rs[mi], sh), __builtin_fmaf(bhi(v[j]), rs[mi], sh));
                }
                af[mi] = __builtin_bit_cast(bf16x8, v);
            }
#pragma unroll
            for (int t = 0; t < NT; ++t) {
                const bf16x8 wf = lds16(lds + (((c % RING) * 4 + kk) * NT + t) * 1024 + lane16);
#pragma unroll
                for (int mi = 0; mi < MW; ++mi) acc[mi][t] = MFMA16(wf, af[mi], acc[mi][t]);
            }
        }
    };
    step(IC(0)); step(IC(1)); step(IC(2)); step(IC(3)); step(IC(4));
    if constexpr (NC == 12) { step(IC(5)); step(IC(6)); step(IC(7)); step(IC(8)); step(IC(9)); step(IC(10)); step(IC(11)); }
    if constexpr (E == E_W3) {
#pragma unroll
        for (int mi = 0; mi < MW; ++mi)
#pragma unroll
            for (int p = 0; p < 3; ++p) {
                const f32x4 u = acc[mi][2 * p], v = acc[mi][2 * p + 1];
                *(u32x4*)(ws + WS_G + (size_t)((mt0 + mi) * 48 + nb * 3 + p) * 1024 + lane16) =
                    (u32x4){pk2(gelu_t(u[0]), gelu_t(u[1])), pk2(gelu_t(u[2]), gelu_t(u[3])), pk2(gelu_t(v[0]), gelu_t(v[1])), pk2(gelu_t(v[2]), gelu_t(v[3]))};
            }
    } else {
        float v[20];
#pragma unroll
        for (int t = 0; t < 5; ++t)
#pragma unroll
            for (int j = 0; j < 4; ++j) v[4 * t + j] = acc[0][t][j];
        if (E == E_W1) {
#pragma unroll
            for (int j = 0; j < 20; ++j) v[j] = gelu_t(v[j]);
        } else {
            const unsigned ru[10] = {ra[0][0], ra[0][1], ra[0][2], ra[0][3], ra[1][0], ra[1][1], ra[1][2], ra[1][3], ra4[0], ra4[1]};
            const float sh = E == E_W2 ? -mu[0] * rs[0] : 0.f;
#pragma unroll
            for (int j = 0; j < 10; ++j) {
                float lo = blo(ru[j]), hi = bhi(ru[j]);
                if (E == E_W2) { lo = __builtin_fmaf(lo, rs[0], sh); hi = __builtin_fmaf(hi, rs[0], sh); }
                v[2 * j] += lo; v[2 * j + 1] += hi;
            }
            if (E == E_W4) {
                const unsigned rv[10] = {rb[0][0], rb[0][1], rb[0][2], rb[0][3], rb[1][0], rb[1][1], rb[1][2], rb[1][3], rb4[0], rb4[1]};
#pragma unroll
                for (int j = 0; j < 10; ++j) { v[2 * j] += blo(rv[j]); v[2 * j + 1] += bhi(rv[j]); }
            }
        }
        unsigned pk[10];
#pragma unroll
        for (int j = 0; j < 10; ++j) pk[j] = pk2(v[2 * j], v[2 * j + 1]);
        if (E == E_W4) {
            char* ob = out + (size_t)(mt0 * 16 + r) * 1280;
            *(u32x4*)(ob + gb * 2 + g * 16) = (u32x4){pk[0], pk[1], pk[2], pk[3]};
            *(u32x4*)(ob + gb * 2 + 64 + g * 16) = (u32x4){pk[4], pk[5], pk[6], pk[7]};
            *(u32x2*)(ob + hb * 2 + g * 8) = (u32x2){pk[8], pk[9]};
        } else {
            int row = mt0 * 16 + r;
            if (E == E_W1) {
                const int cg = nb >> 1, sh = (cg & 1) ? -(1 + (cg >> 1)) : 1 + (cg >> 1);
                row += sh;
                if ((unsigned)row >= (unsigned)S_) {
#pragma unroll
                    for (int j = 0; j < 10; ++j) pk[j] = 0u;
                }
                row &= S_ - 1;
            }
            char* ob = ws + (E == E_WO ? WS_H : E == E_W1 ? WS_HS : WS_X1) + (size_t)(row >> 4) * 20480 + ((row & 15) - r) * 16;
            *(u32x4*)(ob + (gb >> 5) * 1024 + lane16) = (u32x4){pk[0], pk[1], pk[2], pk[3]};
            *(u32x4*)(ob + (gb >> 5) * 1024 + 1024 + lane16) = (u32x4){pk[4], pk[5], pk[6], pk[7]};
            *(u32x2*)(ob + hoff) = (u32x2){pk[8], pk[9]};
            if (E != E_W1) {
                float s1 = 0.f, s2 = 0.f;
#pragma unroll
                for (int j = 0; j < 10; ++j) { const float lo = blo(pk[j]), hi = bhi(pk[j]); s1 += lo + hi; s2 = __builtin_fmaf(lo, lo, __builtin_fmaf(hi, hi, s2)); }
                s1 = xsum32(xsum16(s1)); s2 = xsum32(xsum16(s2));
                if (g == 0) *(f32x2*)(ws + (E == E_WO ? WS_STH : WS_STX) + (size_t)row * 64 + nb * 8) = (f32x2){s1, s2};
            }
        }
    }
}

extern "C" void kernel_launch(void* const* d_in, const int* in_sizes, int n_in, void* d_out, int out_size, void* d_ws, size_t ws_size, hipStream_t stream) {
    const char* x = (const char*)d_in[0];
    char* ws = (char*)d_ws;
    char* out = (char*)d_out;
    WPtrs wp;
    wp.wo = (const char*)d_in[2]; wp.w1 = (const char*)d_in[4]; wp.w2 = (const char*)d_in[5]; wp.w3 = (const char*)d_in[6]; wp.w4 = (const char*)d_in[7];
    (void)hipFuncSetAttribute((const void*)k_qkv, hipFuncAttributeMaxDynamicSharedMemorySize, 163840);
    (void)hipFuncSetAttribute((const void*)k_attn, hipFuncAttributeMaxDynamicSharedMemorySize, AT_LDS);
    (void)hipFuncSetAttribute((const void*)k_chain<E_WO>, hipFuncAttributeMaxDynamicSharedMemorySize, 102400);
    (void)hipFuncSetAttribute((const void*)k_chain<E_W1>, hipFuncAttributeMaxDynamicSharedMemorySize, 102400);
    (void)hipFuncSetAttribute((const void*)k_chain<E_W2>, hipFuncAttributeMaxDynamicSharedMemorySize, 102400);
    (void)hipFuncSetAttribute((const void*)k_chain<E_W3>, hipFuncAttributeMaxDynamicSharedMemorySize, 122880);
    (void)hipFuncSetAttribute((const void*)k_chain<E_W4>, hipFuncAttributeMaxDynamicSharedMemorySize, 143360);
    k_qkv<<<240, 256, 163840, stream>>>(x, (const char*)d_in[1], ws);
    k_attn<<<256, 1024, AT_LDS, stream>>>(wp, ws);
    k_chain<E_WO><<<256, 256, 102400, stream>>>(x, ws, out);
    k_chain<E_W1><<<256, 256, 102400, stream>>>(x, ws, out);
    k_chain<E_W2><<<256, 256, 102400, stream>>>(x, ws, out);
    k_chain<E_W3><<<256, 256, 122880, stream>>>(x, ws, out);
    k_chain<E_W4><<<256, 256, 143360, stream>>>(x, ws, out);
}
```

```cpp
#include <hip/hip_runtime.h>
#include <hip/hip_bf16.h>
#include <stdint.h>
#include <type_traits>

typedef __attribute__((ext_vector_type(8))) short bf16x8;
typedef __attribute__((ext_vector_type(4))) short bf16x4;
typedef __attribute__((ext_vector_type(16))) float f32x16;
typedef __attribute__((ext_vector_type(4))) float f32x4;
typedef __attribute__((ext_vector_type(2))) float f32x2;
typedef __attribute__((ext_vector_type(4))) unsigned u32x4;
typedef __attribute__((ext_vector_type(2))) unsigned u32x2;
typedef __attribute__((ext_vector_type(2))) __bf16 bfv2;
#define LAS __attribute__((address_space(3)))
#define GAS __attribute__((address_space(1)))
#define DI __device__ __forceinline__

constexpr int S_ = 2048, D_ = 640, H_ = 32, F_ = 1536;
constexpr float EPS = 1e-5f;
constexpr float QSCALE = 0.22360679774997896f * 1.4426950408889634f;

constexpr unsigned SZ_ACT = S_ * D_ * 2;
constexpr int KVT = 2688;
constexpr unsigned WS_FL = 0;
constexpr unsigned FL_RT = WS_FL;
constexpr unsigned FL_AT = FL_RT + 1024;
constexpr unsigned FL_ST = FL_AT + 1024;
constexpr unsigned FL_END = FL_ST + 4 * 4096;
constexpr unsigned WS_Q = 32768;
constexpr unsigned WS_KV = WS_Q + SZ_ACT;
constexpr unsigned WS_O = WS_KV + H_ * 64 * KVT;
constexpr unsigned WS_H = WS_O + SZ_ACT;
constexpr unsigned WS_HS = WS_H + SZ_ACT;
constexpr unsigned WS_X1 = WS_HS + SZ_ACT;
constexpr unsigned WS_G = WS_X1 + SZ_ACT;
constexpr unsigned WS_STH = WS_G + S_ * F_ * 2;
constexpr unsigned WS_STX = WS_STH + S_ * 64;
constexpr unsigned WS_CS = WS_STX + S_ * 64;
constexpr unsigned WS_WO = WS_CS + 24 * 20 * 384;
constexpr unsigned WS_W1 = WS_WO + D_ * D_ * 2;
constexpr unsigned WS_W2 = WS_W1 + D_ * D_ * 2;
constexpr unsigned WS_W3 = WS_W2 + D_ * D_ * 2;
constexpr unsigned WS_W4 = WS_W3 + D_ * F_ * 2;
constexpr unsigned WS_END = WS_W4 + D_ * F_ * 2;

DI unsigned pk2(float a, float b) { bfv2 v = {(__bf16)a, (__bf16)b}; return __builtin_bit_cast(unsigned, v); }
DI float blo(unsigned u) { return __uint_as_float(u << 16); }
DI float bhi(unsigned u) { return __uint_as_float(u & 0xffff0000u); }
DI int rfl(int x) { return __builtin_amdgcn_readfirstlane(x); }
DI float xsum32(float v) { auto r = __builtin_amdgcn_permlane32_swap(__float_as_uint(v), __float_as_uint(v), false, false); return __uint_as_float(r[0]) + __uint_as_float(r[1]); }
DI float xsum16(float v) { auto r = __builtin_amdgcn_permlane16_swap(__float_as_uint(v), __float_as_uint(v), false, false); return __uint_as_float(r[0]) + __uint_as_float(r[1]); }
DI float xmax32(float v) { auto r = __builtin_amdgcn_permlane32_swap(__float_as_uint(v), __float_as_uint(v), false, false); return fmaxf(__uint_as_float(r[0]), __uint_as_float(r[1])); }
DI float gelu_t(float x) {
    constexpr float c1 = -2.f * 0.7978845608028654f * 1.4426950408889634f, c2 = c1 * 0.044715f;
    const float e = __builtin_amdgcn_exp2f(x * __builtin_fmaf(x * x, c2, c1));
    return x * __builtin_amdgcn_rcpf(1.f + e);
}
template <int OFF> DI void glds(const void* sbase, unsigned voff, unsigned m0v) {
    asm volatile("s_mov_b32 m0, %2\n\ts_nop 0\n\tglobal_load_lds_dwordx4 %0, %1 offset:%3" ::"v"(voff), "s"(sbase), "s"(m0v), "i"(OFF) : "memory");
}
template <int OFF> DI void glds_dev(const void* sbase, unsigned voff, unsigned m0v) {
    asm volatile("s_mov_b32 m0, %2\n\ts_nop 0\n\tglobal_load_lds_dwordx4 %0, %1 offset:%3 sc1" ::"v"(voff), "s"(sbase), "s"(m0v), "i"(OFF) : "memory");
}
template <int OFF> DI void glds_dev_same(const void* sbase, unsigned voff) {
    asm volatile("global_load_lds_dwordx4 %0, %1 offset:%2 sc1" ::"v"(voff), "s"(sbase), "i"(OFF) : "memory");
}
template <int OFF> DI u32x2 gld8(const void* sbase, unsigned voff) { u32x2 r; asm volatile("global_load_dwordx2 %0, %1, %2 offset:%3" : "=v"(r) : "v"(voff), "s"(sbase), "i"(OFF) : "memory"); return r; }
template <int N> DI void vmwait() { asm volatile("s_waitcnt vmcnt(%0)" ::"i"(N) : "memory"); }
DI void lgbar() { asm volatile("s_waitcnt lgkmcnt(0)\n\ts_barrier" ::: "memory"); }
#define PIN(x) asm volatile("" : "+v"(x))
#define SBAR() __builtin_amdgcn_sched_barrier(0)
DI bf16x8 lds16(const LAS char* p) { return *(const LAS bf16x8*)p; }
DI bf16x4 lds8(const LAS char* p) { return *(const LAS bf16x4*)p; }
DI bf16x4 ldstr(const LAS char* p) { return __builtin_amdgcn_ds_read_tr16_b64_v4i16((LAS bf16x4*)p); }
DI bf16x8 cat8(bf16x4 a, bf16x4 b) { return (bf16x8){a[0], a[1], a[2], a[3], b[0], b[1], b[2], b[3]}; }
#define MFMA32(a, b, c) __builtin_amdgcn_mfma_f32_32x32x16_bf16((a), (b), (c), 0, 0, 0)
#define MFMA32K8(a, b, c) __builtin_amdgcn_mfma_f32_32x32x8bf16_1k((a), (b), (c), 0, 0, 0)
#define MFMA16(a, b, c) __builtin_amdgcn_mfma_f32_16x16x32_bf16((a), (b), (c), 0, 0, 0)
#define IC(n) std::integral_constant<int, (n)>{}
typedef __amdgpu_buffer_rsrc_t rsrc_t;
DI u32x4 ld16d(rsrc_t r, unsigned voff, unsigned soff) { return __builtin_amdgcn_raw_buffer_load_b128(r, voff, soff, 16); }
DI unsigned ld4d(rsrc_t r, unsigned voff, unsigned soff) { return __builtin_amdgcn_raw_buffer_load_b32(r, voff, soff, 16); }
DI void st16d(rsrc_t r, u32x4 v, unsigned voff, unsigned soff) { __builtin_amdgcn_raw_buffer_store_b128(v, r, voff + soff, 0, 16); }
DI void st8d(rsrc_t r, u32x2 v, unsigned voff, unsigned soff) { __builtin_amdgcn_raw_buffer_store_b64(v, r, voff, soff, 16); }
DI void set_flag(char* ws, unsigned off) { __hip_atomic_store((GAS unsigned*)(ws + off), 1u, __ATOMIC_RELAXED, __HIP_MEMORY_SCOPE_AGENT); }
DI void wait_flags(char* ws, unsigned off) {
    GAS unsigned* p = (GAS unsigned*)(ws + off);
    for (unsigned spins = 0; spins < (1u << 20); ++spins) {
        if (__all(__hip_atomic_load(p, __ATOMIC_RELAXED, __HIP_MEMORY_SCOPE_AGENT) == 1u)) break;
        __builtin_amdgcn_s_sleep(1);
    }
    __builtin_amdgcn_fence(__ATOMIC_ACQUIRE, "wavefront");
}

#define STAMP_AT(kid, n)

struct QFrag { bf16x8 x0, x1; bf16x4 w0a, w0b, w1a, w1b; };

template <bool VSEC>
DI void qkv_compute(char* __restrict__ ws, LAS char* lds, int mp, int nt, int wv, int lane) {
    const int wm = wv >> 1, wn = wv & 1, r32 = lane & 31, h = lane >> 5;
    const int row0 = wm * 64 + wn * 32 + r32;
    const unsigned xb = row0 * 128 + ((h ^ ((row0 >> 1) & 7)) << 4);
    const int q = (lane & 15) >> 2;
    const unsigned wb = 16384 + (8 * h + q) * 256 + ((((wn << 1) + wm) ^ q) << 6) + ((lane >> 4) & 1) * 32 + (lane & 3) * 8;
    bf16x8 sel;
    {
        const short one = ((lane & 15) == ((lane >> 4) & 1)) ? (short)0x3F80 : (short)0;
#pragma unroll
        for (int j = 0; j < 8; ++j) sel[j] = one;
    }
    f32x16 a00 = {}, a01 = {}, a10 = {}, a11 = {};
    f32x4 cs = {};
    float sx = 0.f, sxx = 0.f;
    QFrag fa, fb;
#define QRD(F, SO, KK) do { const LAS char* px_ = lds + ((xb ^ ((KK) << 5)) + (SO)); const LAS char* py_ = lds + ((xb ^ ((KK) << 5 | 4096)) + (SO)); \
        const LAS char* pw_ = lds + (wb + (SO)); const LAS char* pv_ = lds + ((wb ^ 64) + (SO)); \
        F.w0a = ldstr(pw_ + (KK) * 4096); F.w0b = ldstr(pw_ + (KK) * 4096 + 1024); F.x0 = lds16(px_); F.x1 = lds16(py_); \
        F.w1a = ldstr(pv_ + (KK) * 4096); F.w1b = ldstr(pv_ + (KK) * 4096 + 1024); SBAR(); } while (0)
#define QMM(A, W, X) A = VSEC ? MFMA32(X, W, A) : MFMA32(W, X, A)
#define QST(U) do { const float lo_ = blo(U), hi_ = bhi(U); sx += lo_; sxx = __builtin_fmaf(lo_, lo_, sxx); sx += hi_; sxx = __builtin_fmaf(hi_, hi_, sxx); \
        asm volatile("" : "+v"(sx), "+v"(sxx)); SBAR(); } while (0)
#define QMATH(F) do { const bf16x8 w0_ = cat8(F.w0a, F.w0b), w1_ = cat8(F.w1a, F.w1b); const u32x4 xu_ = __builtin_bit_cast(u32x4, F.x0); \
        QMM(a00, w0_, F.x0); SBAR(); QST(xu_[0]); QMM(a01, w0_, F.x1); SBAR(); QST(xu_[1]); \
        QMM(a10, w1_, F.x0); SBAR(); QST(xu_[2]); QMM(a11, w1_, F.x1); SBAR(); QST(xu_[3]); cs = MFMA16(sel, w0_, cs); SBAR(); } while (0)
    lgbar();
    STAMP_AT(0, 1);
    QRD(fa, 0, 0);
    unsigned so = 0;
#pragma unroll 1
    for (int c = 0; c < 10; ++c) {
        QRD(fb, so, 1); QMATH(fa);
        QRD(fa, so, 2); QMATH(fb);
        QRD(fb, so, 3); QMATH(fa);
        lgbar();
        so = so == 4 * 32768 ? 0 : so + 32768;
        QRD(fa, so, 0); QMATH(fb);
    }
    STAMP_AT(0, 2);
    LAS float* st_mu = (LAS float*)(lds + 32768); LAS float* st_rs = st_mu + 128; LAS float* st_cs = st_mu + 256;
    {
        sx = xsum32(sx); sxx = xsum32(sxx);
        const float mu = sx * (1.f / D_), var = fmaxf(sxx * (1.f / D_) - mu * mu, 0.f);
        float rs = __builtin_amdgcn_rsqf(var + EPS);
        if (nt < 5) rs *= QSCALE;
        if (h == 0) { st_mu[row0] = mu; st_rs[row0] = rs; }
        if (lane < 16) { st_cs[wn * 64 + wm * 32 + lane] = cs[0]; st_cs[wn * 64 + wm * 32 + 16 + lane] = cs[1]; }
    }
    lgbar();
    if (!VSEC) {
        const bool isq = nt < 5;
        const int nsec = (isq ? nt : nt - 5) * 128;
        const int m0_ = mp * 128 + row0, m1_ = m0_ ^ 32;
        const float mu0 = st_mu[row0], rs0 = st_rs[row0], mu1 = st_mu[row0 ^ 32], rs1 = st_rs[row0 ^ 32];
        const unsigned rb0 = isq ? WS_Q + m0_ * 40 : WS_KV + (m0_ >> 5) * KVT, rb1 = isq ? WS_Q + m1_ * 40 : WS_KV + (m1_ >> 5) * KVT;
        auto quad = [&](const f32x16& u, const f32x16& v, int ni, int q4) {
            const int cl = wn * 64 + ((ni ^ wm) << 5) + 8 * q4 + 4 * h;
            const f32x4 c4 = *(const LAS f32x4*)(st_cs + cl);
            const int qg = (nsec + cl) >> 2, head = (qg * 205) >> 10, qi = qg - head * 5;
            const unsigned off = isq ? head * (S_ * 40) + qi * 8 : head * (64 * KVT) + (qi < 4 ? (qi >> 1) * 512 + r32 * 16 + (qi & 1) * 8 : 1024 + r32 * 8);
            *(u32x2*)(ws + (rb0 + off)) = (u32x2){pk2((u[4 * q4] - mu0 * c4[0]) * rs0, (u[4 * q4 + 1] - mu0 * c4[1]) * rs0), pk2((u[4 * q4 + 2] - mu0 * c4[2]) * rs0, (u[4 * q4 + 3] - mu0 * c4[3]) * rs0)};
            *(u32x2*)(ws + (rb1 + off)) = (u32x2){pk2((v[4 * q4] - mu1 * c4[0]) * rs1, (v[4 * q4 + 1] - mu1 * c4[1]) * rs1), pk2((v[4 * q4 + 2] - mu1 * c4[2]) * rs1, (v[4 * q4 + 3] - mu1 * c4[3]) * rs1)};
        };
#pragma unroll
        for (int q4 = 0; q4 < 4; ++q4) { quad(a00, a01, 0, q4); quad(a10, a11, 1, q4); }
    } else {
        unsigned cb[2]; float cn[2]; int dd[2];
#pragma unroll
        for (int ni = 0; ni < 2; ++ni) {
            const int cl = wn * 64 + ((ni ^ wm) << 5) + r32, n = (nt - 10) * 128 + cl, head = (n * 3277) >> 16, d = n - head * 20;
            cn[ni] = st_cs[cl]; dd[ni] = d;
            cb[ni] = WS_KV + head * (64 * KVT) + 1280 + h * 352 + d * 16;
        }
        auto part = [&](const f32x16& u, const f32x16& v, int mi, int s) {
            const int rl0 = wm * 64 + ((mi ^ wn) << 5);
            const unsigned tb = (mp * 4 + (rl0 >> 5)) * KVT + s * 704;
            u32x4 ou, ov;
#pragma unroll
            for (int qq = 0; qq < 2; ++qq) {
                const int rl = rl0 + 16 * s + 8 * qq + 4 * h, r = 8 * s + 4 * qq;
                const f32x4 mu = *(const LAS f32x4*)(st_mu + rl), rs = *(const LAS f32x4*)(st_rs + rl);
                ou[2 * qq] = pk2((u[r] - mu[0] * cn[0]) * rs[0], (u[r + 1] - mu[1] * cn[0]) * rs[1]);
                ou[2 * qq + 1] = pk2((u[r + 2] - mu[2] * cn[0]) * rs[2], (u[r + 3] - mu[3] * cn[0]) * rs[3]);
                ov[2 * qq] = pk2((v[r] - mu[0] * cn[1]) * rs[0], (v[r + 1] - mu[1] * cn[1]) * rs[1]);
                ov[2 * qq + 1] = pk2((v[r + 2] - mu[2] * cn[1]) * rs[2], (v[r + 3] - mu[3] * cn[1]) * rs[3]);
            }
            *(u32x4*)(ws + (cb[0] + tb)) = ou;
            *(u32x4*)(ws + (cb[1] + tb)) = ov;
        };
        part(a00, a10, 0, 0); part(a00, a10, 0, 1); part(a01, a11, 1, 0); part(a01, a11, 1, 1);
#pragma unroll
        for (int ni = 0; ni < 2; ++ni)
            if (dd[ni] < 2) {
                const unsigned f = dd[ni] == 0 ? 0x3F803F80u : 0u;
#pragma unroll
                for (int i = 0; i < 4; ++i) *(u32x4*)(ws + (cb[ni] + 320 + (mp * 4 + wm * 2 + (i >> 1)) * KVT + (i & 1) * 704)) = (u32x4){f, f, f, f};
            }
    }
}

__global__ void __launch_bounds__(512, 2) k_qkv(const char* __restrict__ x, const char* __restrict__ w, char* __restrict__ ws) {
    extern __shared__ __attribute__((aligned(16))) char smem[];
    LAS char* lds = (LAS char*)smem;
    const int lane = threadIdx.x & 63, wv = rfl(threadIdx.x >> 6);
    const int id = (blockIdx.x & 7) * 30 + (blockIdx.x >> 3);
    const int rg = id / 60, rem = id - rg * 60, nt = rem >> 2, mp = rg * 4 + (rem & 3);
    if (wv < 4) {
        STAMP_AT(0, 0);
        if (nt >= 10) qkv_compute<true>(ws, lds, mp, nt, wv, lane);
        else qkv_compute<false>(ws, lds, mp, nt, wv, lane);
        STAMP_AT(0, 3);
    } else {
        const int lw = wv - 4;
        const unsigned lds0 = (unsigned)(uintptr_t)lds + lw * 4096;
        const unsigned xo0 = (lane >> 3) * 1280 + (((lane & 7) ^ (lane >> 4)) << 4), xo1 = xo0 ^ 64;
        const unsigned wo_ = (lane >> 4) * 3840 + (((lane & 15) ^ ((lane >> 4) << 2)) << 4);
        const char* xa = x + (size_t)(mp * 128 + lw * 32) * 1280;
        const char* wa = w + (size_t)(lw * 16) * 3840 + nt * 256;
        unsigned sl = lds0;
        auto stage = [&]() {
#pragma unroll
            for (int i = 0; i < 4; ++i) {
                glds<0>(xa + i * 10240, (i & 1) ? xo1 : xo0, sl + i * 1024);
                glds<0>(wa + i * 15360, wo_, sl + 16384 + i * 1024);
            }
            xa += 128; wa += 64 * 3840; sl = sl == lds0 + 4 * 32768 ? lds0 : sl + 32768;
        };
#pragma unroll 1
        for (int c = 0; c < 5; ++c) stage();
        vmwait<32>(); lgbar();
#pragma unroll 1
        for (int c = 0; c < 5; ++c) { vmwait<24>(); lgbar(); stage(); }
        vmwait<24>(); lgbar(); vmwait<16>(); lgbar(); vmwait<8>(); lgbar(); vmwait<0>(); lgbar();
        if (blockIdx.x < (FL_END + 4095) / 4096) *(u32x4*)(ws + WS_FL + blockIdx.x * 4096 + (threadIdx.x - 256) * 16) = (u32x4){0u, 0u, 0u, 0u};
        lgbar(); lgbar();
    }
}

constexpr int AT_SLOT = 16 * KVT;
constexpr int AT_RT = 2 * AT_SLOT;
constexpr int L_C1 = 158 * 1024, L_C3 = L_C1 + 384, L_BAD = L_C3 + 384;
constexpr int HS_ = 51200;

struct Args { const char* x; const char* wo; const char* w1; const char* w2; const char* w3; const char* w4; char* ws; char* out; };

DI void at_stage(const char* kv, unsigned lane16, unsigned lds0, int c, int wv) {
    const char* src = kv + c * AT_SLOT + wv * 1024;
    const unsigned dst = lds0 + ((c & 1) ^ 1) * AT_SLOT + wv * 1024;
#pragma unroll
    for (int i = 0; i < 5; ++i) glds<0>(src + i * 8192, lane16, dst + i * 8192);
    if (wv < 2) glds<0>(src + 40960, lane16, dst + 40960);
}
template <int NT> DI void load_steps(const char* src, unsigned dst, int first, int step, int n, unsigned lane16) {
#pragma unroll 1
    for (int j = first; j < n; j += step) {
        const char* s = src + j * (NT * 1024);
        const unsigned d = dst + j * (NT * 1024);
        glds_dev<0>(s, lane16, d); glds_dev_same<1024>(s, lane16); glds_dev_same<2048>(s, lane16); glds_dev_same<3072>(s, lane16);
        glds_dev<0>(s + 4096, lane16, d + 4096);
        if (NT == 6) glds_dev_same<1024>(s + 4096, lane16);
    }
}

template <bool MAXED, typename SIDE>
DI void attn_pass(LAS char* lds, unsigned ka, unsigned kb, unsigned va, bf16x8 qa1, bf16x4 qa2, bf16x8 qb1, bf16x4 qb2, float ma, float mb,
                  f32x16& oa, f32x16& ob, SIDE side) {
    f32x16 ca, cb;
#pragma unroll
    for (int r = 0; r < 16; ++r) { ca[r] = MAXED ? -ma : 0.f; cb[r] = MAXED ? -mb : 0.f; }
    bf16x8 k1 = lds16(lds + AT_SLOT + ka); bf16x4 k2 = lds8(lds + AT_SLOT + kb);
    f32x16 sa = MFMA32(k1, qa1, ca), sb;
    sa = MFMA32K8(k2, qa2, sa);
#define EXP4(A, B, S, I) unsigned A = pk2(__builtin_amdgcn_exp2f(S[I]), __builtin_amdgcn_exp2f(S[I + 1])), \
                                  B = pk2(__builtin_amdgcn_exp2f(S[I + 2]), __builtin_amdgcn_exp2f(S[I + 3])); asm volatile("" : "+v"(A), "+v"(B)); SBAR()
#define HALF(SC, SN, K1, K2, Q1, Q2, CI, O) { \
        EXP4(w0, w1, SC, 0);  SN = MFMA32(K1, Q1, CI); SBAR(); \
        EXP4(w2, w3, SC, 4);  SN = MFMA32K8(K2, Q2, SN); SBAR(); \
        EXP4(w4, w5, SC, 8);  O = MFMA32(v0, __builtin_bit_cast(bf16x8, (u32x4){w0, w1, w2, w3}), O); SBAR(); \
        EXP4(w6, w7, SC, 12); O = MFMA32(v1, __builtin_bit_cast(bf16x8, (u32x4){w4, w5, w6, w7}), O); SBAR(); }
#define TILE(t) do { \
        const bf16x8 v0 = lds16(sl + va + (t) * KVT), v1 = lds16(sl + va + (t) * KVT + 704); \
        bf16x8 n1; bf16x4 n2; \
        if ((t) < 7) { n1 = lds16(sl + ka + ((t) + 1) * KVT); n2 = lds8(sl + kb + ((t) + 1) * KVT); } \
        SBAR(); \
        HALF(sa, sb, k1, k2, qb1, qb2, cb, oa) \
        side(c, IC(t)); \
        if ((t) == 7) { n1 = lds16(sn_ + ka); n2 = lds8(sn_ + kb); SBAR(); } \
        HALF(sb, sa, n1, n2, qa1, qa2, ca, ob) \
        k1 = n1; k2 = n2; } while (0)
#pragma unroll 1
    for (int c = 0; c < 4; ++c) {
        const LAS char* sl = lds + ((c & 1) ^ 1) * AT_SLOT;
        const LAS char* sn_ = lds + (c & 1) * AT_SLOT;
        TILE(0); TILE(1); TILE(2); TILE(3); TILE(4); TILE(5); TILE(6); TILE(7);
    }
}

DI void row_stats(u32x4 st, float& mu, float& var) {
    float a = __uint_as_float(st[0]) + __uint_as_float(st[2]), q = __uint_as_float(st[1]) + __uint_as_float(st[3]);
    a = xsum32(xsum16(a)); q = xsum32(xsum16(q));
    mu = a * (1.f / D_);
    var = fmaxf(q * (1.f / D_) - mu * mu, 0.f);
}

__global__ void __launch_bounds__(512, 2) k_main(Args A) {
    extern __shared__ __attribute__((aligned(16))) char smem[];
    LAS char* lds = (LAS char*)smem;
    char* ws = A.ws;
    const unsigned lds0 = (unsigned)(uintptr_t)lds;
    const int tid = threadIdx.x, lane = tid & 63, wv = rfl(tid >> 6);
    const int b = blockIdx.x, xg = b & 7, bi = b >> 3;
    const unsigned lane16 = lane * 16;
    const rsrc_t R = __builtin_amdgcn_make_buffer_rsrc(ws, 0, WS_END, 0x00020000);
    STAMP_AT(1, 0);
    {
        const int r32 = lane & 31, h = lane >> 5;
        const int head = xg * 4 + (bi >> 3), qb = bi & 7, qt = wv & 3, half = wv >> 2;
        const char* kv = ws + WS_KV + (size_t)head * 64 * KVT;
        at_stage(kv, lane16, lds0, 0, wv);
        const int qrow = qb * 256 + qt * 64 + r32;
        bf16x8 qa1, qb1; bf16x4 qa2, qb2;
        {
            const char* qp = ws + WS_Q + (size_t)head * S_ * 40;
            const unsigned qo = qrow * 40;
            u32x2 a = gld8<0>(qp, qo + h * 16), c = gld8<8>(qp, qo + h * 16), e = gld8<32>(qp, qo);
            u32x2 a2 = gld8<1280>(qp, qo + h * 16), c2 = gld8<1288>(qp, qo + h * 16), e2 = gld8<1312>(qp, qo);
            if (tid == 0) *(LAS unsigned*)(lds + L_BAD) = 0u;
            asm volatile("s_waitcnt vmcnt(0)" : "+v"(a), "+v"(c), "+v"(e), "+v"(a2), "+v"(c2), "+v"(e2)::"memory");
            qa1 = __builtin_bit_cast(bf16x8, (u32x4){a[0], a[1], c[0], c[1]});
            qb1 = __builtin_bit_cast(bf16x8, (u32x4){a2[0], a2[1], c2[0], c2[1]});
            if (h) { e = (u32x2){0u, 0u}; e2 = e; }
            qa2 = __builtin_bit_cast(bf16x4, e); qb2 = __builtin_bit_cast(bf16x4, e2);
        }
        lgbar();
        STAMP_AT(1, 1);
        at_stage(kv, lane16, lds0, 1, wv);
        const int unit = wv * 256 + b;
        const bool rt = unit < 1184;
        int mat = 0, rt_nb = 0, rt_ks = 0;
        if (rt) {
            if (unit < 480) { mat = unit / 160; const int u = unit - mat * 160; rt_nb = u / 20; rt_ks = u - rt_nb * 20; }
            else if (unit < 800) { mat = 3; const int u = unit - 480; rt_nb = u / 20; rt_ks = u - rt_nb * 20; }
            else { mat = 4; const int u = unit - 800; rt_nb = u / 48; rt_ks = u - rt_nb * 48; }
            const int nt = mat == 3 ? 6 : 5, pitch = mat == 3 ? 3072 : 1280;
            const char* src = (mat == 0 ? A.wo : mat == 1 ? A.w1 : mat == 2 ? A.w2 : mat == 3 ? A.w3 : A.w4) + rt_nb * nt * 32;
#pragma unroll 1
            for (int i = 0; i < nt; ++i) {
                const int e = i * 64 + lane, rr = mat == 3 ? (e * 5462) >> 16 : (e * 6554) >> 16, cc = e - rr * nt * 2;
                int k = rt_ks * 32 + rr;
                if (mat == 0) {
                    const int sg = k >> 3, j = k & 7, p = (sg * 205) >> 10, u = sg - p * 5;
                    k = 40 * p + (u == 4 ? 16 + (j >> 2) * 20 : (u >> 1) * 20 + (u & 1) * 4 + (j >> 2) * 8) + (j & 3);
                }
                glds<0>(src, k * pitch + cc * 16, lds0 + AT_RT + wv * 6144 + i * 1024);
            }
        }
        const unsigned ho = half * 8 * KVT;
        const unsigned ka = lane16 + ho, kb = (h ? 1280 + 21 * 16 : r32 * 8 + 1024) + ho, va = 1280 + h * 352 + (r32 < 21 ? r32 : 21) * 16 + ho;
        f32x16 oa = {}, ob = {};
        STAMP_AT(1, 2);
        attn_pass<false>(lds, ka, kb, va, qa1, qa2, qb1, qb2, 0.f, 0.f, oa, ob, [&](int c, auto tt) {
            constexpr int t = decltype(tt)::value;
            if constexpr (t == 7) {
                vmwait<0>(); lgbar();
                if (c < 2) at_stage(kv, lane16, lds0, c + 2, wv);
                if (c == 1 && tid == 0) set_flag(ws, FL_RT + b * 4);
                SBAR();
            }
            if constexpr (t == 0) {
                if (c == 1 && rt) {
                    const int nt = mat == 3 ? 6 : 5, pitch = nt * 32, g = lane >> 4, q = (lane & 15) >> 2, p = lane & 3;
                    const unsigned dst = (mat == 0 ? WS_WO : mat == 1 ? WS_W1 : mat == 2 ? WS_W2 : mat == 3 ? WS_W3 : WS_W4) + (rt_nb * (mat == 4 ? 48 : 20) + rt_ks) * nt * 1024;
                    const unsigned cdst = WS_CS + ((mat == 3 ? 8 + rt_nb : rt_nb) * 20 + rt_ks) * 384;
                    const LAS char* st = lds + AT_RT + wv * 6144 + (8 * g + q) * pitch;
                    const bf16x8 ones = {0x3F80, 0x3F80, 0x3F80, 0x3F80, 0x3F80, 0x3F80, 0x3F80, 0x3F80};
#pragma unroll 1
                    for (int u = 0; u < nt; ++u) {
                        int col;
                        if (nt == 6) col = (u >> 1) * 32 + 8 * p + 4 * (u & 1);
                        else if (u == 4) col = ((rt_nb & 1) ? 0 : 64) + 4 * p;
                        else col = ((rt_nb & 1) ? 16 : 0) + (u >> 1) * 32 + 8 * p + 4 * (u & 1);
                        const bf16x8 f = cat8(ldstr(st + col * 2), ldstr(st + col * 2 + 4 * pitch));
                        st16d(R, __builtin_bit_cast(u32x4, f), lane16, dst + u * 1024);
                        if (mat == 1 || mat == 3) {
                            const f32x4 cs = MFMA16(f, ones, (f32x4){});
                            if ((lane & 15) == 0) st16d(R, __builtin_bit_cast(u32x4, cs), g * 16, cdst + u * 64);
                        }
                    }
                    SBAR();
                }
            }
            if constexpr (t == 4) {
                if (c == 2 && wv == 0) {
                    u32x4 f;
                    for (unsigned spins = 0; spins < (1u << 20); ++spins) {
                        f = ld16d(R, lane16, FL_RT);
                        if (__all((f[0] & f[1] & f[2] & f[3]) == 1u)) break;
                        __builtin_amdgcn_s_sleep(1);
                    }
                    __builtin_amdgcn_fence(__ATOMIC_ACQUIRE, "agent");
                    SBAR();
                }
            }
            if constexpr (t < 3) {
                if (c == 3) { load_steps<5>(ws + WS_WO + (bi & 7) * 102400, lds0 + HS_, t * 8 + wv, 20, 20, lane16); SBAR(); }
            }
        });
        STAMP_AT(1, 3);
        for (int attempt = 0;; ++attempt) {
            LAS float* cbuf = (LAS float*)lds + qt * 24 * 64 + lane;
            if (half) {
#pragma unroll
                for (int r = 0; r < 12; ++r) { cbuf[r * 64] = oa[r]; cbuf[(12 + r) * 64] = ob[r]; }
            }
            lgbar();
            if (!half) {
                const unsigned sg = (5 * (head >> 1) + 2 * (head & 1) + h) * 256, s4 = (5 * (head >> 1) + 4) * 256 + (head & 1) * 8;
                bool ok = true;
#pragma unroll
                for (int i = 0; i < 2; ++i) {
                    float o[12];
#pragma unroll
                    for (int r = 0; r < 12; ++r) o[r] = (i ? ob[r] : oa[r]) + cbuf[(12 * i + r) * 64];
                    auto lr = __builtin_amdgcn_permlane32_swap(__float_as_uint(o[8]), __float_as_uint(o[8]), false, false);
                    const float l = __uint_as_float(lr[1]);
                    ok = ok && l >= 0x1p-60f && l <= 0x1p60f;
                    const float inv = 1.f / l;
                    const int row = qrow + 32 * i;
                    const unsigned ob_ = (row >> 4) * 20480 + (row & 15) * 16;
                    st16d(R, (u32x4){pk2(o[0] * inv, o[1] * inv), pk2(o[2] * inv, o[3] * inv), pk2(o[4] * inv, o[5] * inv), pk2(o[6] * inv, o[7] * inv)}, ob_ + sg, WS_O);
                    if (!h) st8d(R, (u32x2){pk2(o[8] * inv, o[9] * inv), pk2(o[10] * inv, o[11] * inv)}, ob_ + s4, WS_O);
                }
                if (!__all(ok) && lane == 0 && attempt == 0) *(LAS unsigned*)(lds + L_BAD) = 1u;
            }
            vmwait<0>(); lgbar();
            if (attempt == 1 || rfl(*(const LAS unsigned*)(lds + L_BAD)) == 0) break;
            float ma = -INFINITY, mb = -INFINITY;
            at_stage(kv, lane16, lds0, 0, wv);
#pragma unroll 1
            for (int c = 0; c < 4; ++c) {
                vmwait<0>(); lgbar();
                if (c < 3) at_stage(kv, lane16, lds0, c + 1, wv);
                const LAS char* sl = lds + ((c & 1) ^ 1) * AT_SLOT;
#pragma unroll 1
                for (int t = 0; t < 8; ++t) {
                    const bf16x8 k1 = lds16(sl + ka + t * KVT); const bf16x4 k2 = lds8(sl + kb + t * KVT);
                    f32x16 s = MFMA32(k1, qa1, (f32x16){}), u = MFMA32(k1, qb1, (f32x16){});
                    s = MFMA32K8(k2, qa2, s); u = MFMA32K8(k2, qb2, u);
#pragma unroll
                    for (int r = 0; r < 16; ++r) { ma = fmaxf(ma, s[r]); mb = fmaxf(mb, u[r]); }
                }
            }
            ma = xmax32(ma); mb = xmax32(mb);
            LAS float* mbuf = (LAS float*)(lds + AT_SLOT) + qt * 128 + r32;
            if (!h) { mbuf[half * 64] = ma; mbuf[half * 64 + 32] = mb; }
            lgbar();
            ma = fmaxf(mbuf[0], mbuf[64]); mb = fmaxf(mbuf[32], mbuf[96]);
            lgbar();
            at_stage(kv, lane16, lds0, 0, wv);
            vmwait<0>(); lgbar();
            at_stage(kv, lane16, lds0, 1, wv);
            oa = (f32x16){}; ob = (f32x16){};
            attn_pass<true>(lds, ka, kb, va, qa1, qa2, qb1, qb2, ma, mb, oa, ob, [&](int c, auto tt) {
                constexpr int t = decltype(tt)::value;
                if constexpr (t == 7) { vmwait<0>(); lgbar(); if (c < 2) at_stage(kv, lane16, lds0, c + 2, wv); SBAR(); }
                if constexpr (t < 3) { if (c == 3) { load_steps<5>(ws + WS_WO + (bi & 7) * 102400, lds0 + HS_, t * 8 + wv, 20, 20, lane16); SBAR(); } }
            });
        }
        if (tid == 256) set_flag(ws, FL_AT + qb * 128 + head * 4);
    }
    STAMP_AT(1, 4);
    const int nb = bi & 7, panel = xg * 4 + (bi >> 3), nb3 = bi & 15, panel3 = xg * 2 + (bi >> 4);
    if (wv >= 4) {
        const int lw = wv - 4;
        const char* wsl = ws + nb * 102400;
#pragma unroll 1
        for (int st = 0; st < 3; ++st) {
            if (lw == 0) {
                const unsigned f = st == 0 ? FL_AT + xg * 128 + (lane & 31) * 4
                                 : st == 1 ? FL_ST + panel * 128 + (lane & 7) * 4
                                           : FL_ST + 4096 + ((panel + 31 + ((lane >> 3) & 3) % 3) & 31) * 128 + (lane & 7) * 4;
                wait_flags(ws, f);
            }
            lgbar();
            lgbar();
            if (st == 0) {
                load_steps<5>(wsl + WS_W1, lds0, lw, 4, 10, lane16); load_steps<5>(wsl + WS_W1 + HS_, lds0 + HS_, lw, 4, 10, lane16);
                const int j = lw * 64 + lane, c = j < 96 ? j : j - 96;
                if (j < 192) {
                    const unsigned src = WS_CS + (j < 96 ? nb : 8 + nb3) * 7680 + c * 4;
                    float s = 0.f;
#pragma unroll
                    for (int k = 0; k < 20; ++k) s += __uint_as_float(ld4d(R, src + k * 384, 0));
                    *(LAS float*)(lds + L_C1 + j * 4) = s;
                }
            } else if (st == 1) {
                load_steps<5>(wsl + WS_W2, lds0 + 2 * HS_, lw, 4, 10, lane16); load_steps<5>(wsl + WS_W2 + HS_, lds0, lw, 4, 10, lane16);
            } else {
                load_steps<6>(ws + WS_W3 + nb3 * 122880, lds0 + HS_, lw, 4, 18, lane16);
            }
            lgbar();
            if (tid == 256) set_flag(ws, FL_ST + st * 4096 + panel * 128 + nb * 4);
            if (st == 2) load_steps<6>(ws + WS_W3 + nb3 * 122880 + 18 * 6144, lds0, lw, 4, 2, lane16);
            vmwait<0>();
        }
        const char* w4 = ws + WS_W4 + nb * 245760;
        if (lw == 0) wait_flags(ws, FL_ST + 2 * 4096 + (panel3 * 2 + ((lane >> 3) & 1)) * 128 + (lane & 7) * 4);
        lgbar();
        lgbar();
        load_steps<5>(w4, lds0 + 3 * 5120, lw, 4, 19, lane16);
        lgbar();
        load_steps<5>(w4 + 19 * 5120, lds0 + 22 * 5120, lw, 4, 10, lane16); load_steps<5>(w4 + 29 * 5120, lds0, lw, 4, 3, lane16);
        lgbar();
        if (tid == 256) set_flag(ws, FL_ST + 3 * 4096 + panel3 * 128 + nb3 * 4);
        vmwait<0>();
        if (lw == 0) wait_flags(ws, FL_ST + 3 * 4096 + (panel >> 1) * 128 + (lane & 15) * 4);
        lgbar();
        lgbar();
        load_steps<5>(w4 + 32 * 5120, lds0 + 3 * 5120, lw, 4, 8, lane16);
        lgbar();
        load_steps<5>(w4 + 40 * 5120, lds0 + 11 * 5120, lw, 4, 8, lane16);
        vmwait<10>(); lgbar();
        vmwait<0>(); lgbar();
        return;
    }
    const int r = lane & 15, g = lane >> 4;
    const int mt = panel * 4 + wv;
    const int gb = nb * 80 + ((nb & 1) ? 16 : 0), hb = nb * 80 + ((nb & 1) ? 0 : 64);
    const unsigned foff = (gb >> 5) * 1024 + lane16, hoff = ((hb >> 5) * 64 + (((hb & 31) >> 3) + (g >> 1)) * 16 + r) * 16 + (g & 1) * 8;
    unsigned res[10];
    {
        const char* xb = A.x + (size_t)(mt * 16 + r) * 1280;
        const u32x4 u = *(const u32x4*)(xb + gb * 2 + g * 16), v = *(const u32x4*)(xb + gb * 2 + 64 + g * 16); const u32x2 w = *(const u32x2*)(xb + hb * 2 + g * 8);
        res[0] = u[0]; res[1] = u[1]; res[2] = u[2]; res[3] = u[3]; res[4] = v[0]; res[5] = v[1]; res[6] = v[2]; res[7] = v[3]; res[8] = w[0]; res[9] = w[1];
#pragma unroll
        for (int j = 0; j < 10; ++j) PIN(res[j]);
    }
    float hx[20];
    float mu = 0.f, var = 0.f;
#pragma unroll 1
    for (int st = 0; st < 3; ++st) {
        constexpr int PRE = 9;
        const unsigned ab = (st == 0 ? WS_O : st == 1 ? WS_H : WS_HS) + mt * 20480;
        const LAS char* b0 = lds + (st == 0 ? HS_ : st == 1 ? 0 : 2 * HS_) + lane16;
        const LAS char* b1 = lds + (st == 0 ? 2 * HS_ : st == 1 ? HS_ : 0) + lane16;
        lgbar();
        STAMP_AT(1, 5 + 4 * st);
        u32x4 a[20], stt;
        bf16x8 bw[3][5];
        f32x4 acc[5] = {};
#pragma unroll
        for (int k = 0; k < PRE; ++k) { a[k] = ld16d(R, lane16, ab + k * 1024); SBAR(); }
        stt = ld16d(R, r * 64 + g * 16, st == 1 ? WS_STH + mt * 1024 : ab);
#pragma unroll
        for (int k = 0; k < 2; ++k)
#pragma unroll
            for (int t = 0; t < 5; ++t) bw[k][t] = lds16(b0 + (k * 5 + t) * 1024);
        SBAR();
#pragma unroll
        for (int k = 0; k < 20; ++k) {
            if (k + PRE < 20) a[k + PRE] = ld16d(R, lane16, ab + (k + PRE) * 1024);
            SBAR();
#pragma unroll
            for (int t = 0; t < 5; ++t) {
                if (k + 2 < 20) bw[(k + 2) % 3][t] = lds16((k + 2 < 10 ? b0 + (k + 2) * 5120 : b1 + (k - 8) * 5120) + t * 1024);
                acc[t] = MFMA16(bw[k % 3][t], __builtin_bit_cast(bf16x8, a[k]), acc[t]);
                SBAR();
            }
            if (k == 10) lgbar();
        }
        STAMP_AT(1, 6 + 4 * st);
        float v[20];
#pragma unroll
        for (int j = 0; j < 20; ++j) v[j] = acc[j >> 2][j & 3];
        int row = mt * 16 + r;
        unsigned pk[10];
        if (st == 1) {
            row_stats(stt, mu, var);
            const float r1 = __builtin_amdgcn_rsqf(var + EPS);
            const float rs = r1 * __builtin_amdgcn_rsqf(var * r1 * r1 + EPS);
#pragma unroll
            for (int t = 0; t < 5; ++t) {
                const f32x4 c = *(const LAS f32x4*)(lds + L_C1 + t * 64 + g * 16);
#pragma unroll
                for (int j = 0; j < 4; ++j) v[4 * t + j] = gelu_t((v[4 * t + j] - mu * c[j]) * rs);
            }
#pragma unroll
            for (int j = 0; j < 10; ++j) pk[j] = pk2(v[2 * j], v[2 * j + 1]);
            const int cg = nb >> 1;
            row += (cg & 1) ? -(1 + (cg >> 1)) : 1 + (cg >> 1);
            if ((unsigned)row >= (unsigned)S_) {
#pragma unroll
                for (int j = 0; j < 10; ++j) pk[j] = 0u;
            }
            row &= S_ - 1;
        } else {
            const float rs = st == 0 ? 1.f : __builtin_amdgcn_rsqf(var + EPS), sh = st == 0 ? 0.f : -mu * rs;
#pragma unroll
            for (int j = 0; j < 10; ++j) pk[j] = pk2(v[2 * j] + __builtin_fmaf(blo(res[j]), rs, sh), v[2 * j + 1] + __builtin_fmaf(bhi(res[j]), rs, sh));
        }
        const unsigned ob = (st == 0 ? WS_H : st == 1 ? WS_HS : WS_X1), ro = (row >> 4) * 20480 + ((row & 15) - r) * 16;
        st16d(R, (u32x4){pk[0], pk[1], pk[2], pk[3]}, ro + foff, ob);
        st16d(R, (u32x4){pk[4], pk[5], pk[6], pk[7]}, ro + foff + 1024, ob);
        st8d(R, (u32x2){pk[8], pk[9]}, ro + hoff, ob);
        if (st != 1) {
            float s1 = 0.f, s2 = 0.f;
#pragma unroll
            for (int j = 0; j < 10; ++j) {
                const float lo = blo(pk[j]), hi = bhi(pk[j]);
                s1 += lo + hi; s2 = __builtin_fmaf(lo, lo, __builtin_fmaf(hi, hi, s2));
                hx[2 * j] = lo + blo(res[j]); hx[2 * j + 1] = hi + bhi(res[j]);
                if (st == 0) res[j] = pk[j];
            }
            s1 = xsum32(xsum16(s1)); s2 = xsum32(xsum16(s2));
            if (g == 0) st8d(R, (u32x2){__float_as_uint(s1), __float_as_uint(s2)}, row * 64 + nb * 8, st == 0 ? WS_STH : WS_STX);
        }
        STAMP_AT(1, 7 + 4 * st);
        vmwait<0>(); lgbar();
        STAMP_AT(1, 8 + 4 * st);
    }
    {
        constexpr int RA = 12, PRE = 6;
        const int mt3 = panel3 * 8 + wv * 2;
        const unsigned ab = WS_X1 + mt3 * 20480;
        const LAS char* bp = lds + lane16;
        lgbar();
        STAMP_AT(1, 17);
        u32x4 a[RA][2], stt[2];
        bf16x8 bw[2][6];
        f32x4 acc[2][6] = {};
#pragma unroll
        for (int k = 0; k < PRE; ++k) { a[k][0] = ld16d(R, lane16, ab + k * 1024); a[k][1] = ld16d(R, lane16, ab + 20480 + k * 1024); SBAR(); }
        stt[0] = ld16d(R, r * 64 + g * 16, WS_STX + mt3 * 1024); stt[1] = ld16d(R, r * 64 + g * 16, WS_STX + mt3 * 1024 + 1024);
#pragma unroll
        for (int t = 0; t < 6; ++t) bw[0][t] = lds16(bp + HS_ + t * 1024);
        SBAR();
#pragma unroll
        for (int k = 0; k < 20; ++k) {
            if (k + PRE < 20) { a[(k + PRE) % RA][0] = ld16d(R, lane16, ab + (k + PRE) * 1024); a[(k + PRE) % RA][1] = ld16d(R, lane16, ab + 20480 + (k + PRE) * 1024); }
            SBAR();
#pragma unroll
            for (int t = 0; t < 6; ++t) {
                if (k + 1 < 20) bw[(k + 1) & 1][t] = lds16(bp + (k + 1 < 18 ? HS_ + (k + 1) * 6144 : (k - 17) * 6144) + t * 1024);
                acc[0][t] = MFMA16(bw[k & 1][t], __builtin_bit_cast(bf16x8, a[k % RA][0]), acc[0][t]);
                acc[1][t] = MFMA16(bw[k & 1][t], __builtin_bit_cast(bf16x8, a[k % RA][1]), acc[1][t]);
                SBAR();
            }
            if (k == 9) lgbar();
        }
        f32x4 c3[6];
#pragma unroll
        for (int t = 0; t < 6; ++t) c3[t] = *(const LAS f32x4*)(lds + L_C3 + t * 64 + g * 16);
        lgbar();
        STAMP_AT(1, 18);
#pragma unroll
        for (int mi = 0; mi < 2; ++mi) {
            float m3, v3;
            row_stats(stt[mi], m3, v3);
            const float rs = __builtin_amdgcn_rsqf(v3 + EPS);
#pragma unroll
            for (int p = 0; p < 3; ++p) {
                float y[8];
#pragma unroll
                for (int j = 0; j < 8; ++j) y[j] = gelu_t((acc[mi][2 * p + (j >> 2)][j & 3] - m3 * c3[2 * p + (j >> 2)][j & 3]) * rs);
                st16d(R, (u32x4){pk2(y[0], y[1]), pk2(y[2], y[3]), pk2(y[4], y[5]), pk2(y[6], y[7])}, lane16, WS_G + ((mt3 + mi) * 48 + nb3 * 3 + p) * 1024);
            }
        }
        STAMP_AT(1, 19);
        vmwait<0>(); lgbar();
        STAMP_AT(1, 20);
    }
    {
        constexpr int RA = 16, PRE = 10;
        const unsigned ab = WS_G + mt * 49152;
        const LAS char* bp = lds + lane16;
        lgbar();
        STAMP_AT(1, 21);
        u32x4 a[RA];
        bf16x8 bw[3][5];
        f32x4 acc[5] = {};
#pragma unroll
        for (int k = 0; k < PRE; ++k) { a[k] = ld16d(R, lane16, ab + k * 1024); SBAR(); }
#pragma unroll
        for (int k = 0; k < 2; ++k)
#pragma unroll
            for (int t = 0; t < 5; ++t) bw[k][t] = lds16(bp + (k + 3) * 5120 + t * 1024);
        SBAR();
#pragma unroll
        for (int k = 0; k < 48; ++k) {
            if (k + PRE < 48) a[(k + PRE) % RA] = ld16d(R, lane16, ab + (k + PRE) * 1024);
            if (k == 6 || k == 14 || k == 30 || k == 38) lgbar();
            SBAR();
#pragma unroll
            for (int t = 0; t < 5; ++t) {
                if (k + 2 < 48) bw[(k + 2) % 3][t] = lds16(bp + ((k + 5) & 31) * 5120 + t * 1024);
                acc[t] = MFMA16(bw[k % 3][t], __builtin_bit_cast(bf16x8, a[k % RA]), acc[t]);
                SBAR();
            }
        }
        STAMP_AT(1, 22);
        unsigned pk[10];
#pragma unroll
        for (int j = 0; j < 10; ++j) pk[j] = pk2(acc[j >> 1][(2 * j) & 3] + hx[2 * j], acc[j >> 1][(2 * j + 1) & 3] + hx[2 * j + 1]);
        char* ob = A.out + (size_t)(mt * 16 + r) * 1280;
        *(u32x4*)(ob + gb * 2 + g * 16) = (u32x4){pk[0], pk[1], pk[2], pk[3]};
        *(u32x4*)(ob + gb * 2 + 64 + g * 16) = (u32x4){pk[4], pk[5], pk[6], pk[7]};
        *(u32x2*)(ob + hb * 2 + g * 8) = (u32x2){pk[8], pk[9]};
    }
}

extern "C" void kernel_launch(void* const* d_in, const int* in_sizes, int n_in, void* d_out, int out_size, void* d_ws, size_t ws_size, hipStream_t stream) {
    Args a;
    a.x = (const char*)d_in[0]; a.wo = (const char*)d_in[2]; a.w1 = (const char*)d_in[4]; a.w2 = (const char*)d_in[5];
    a.w3 = (const char*)d_in[6]; a.w4 = (const char*)d_in[7]; a.ws = (char*)d_ws; a.out = (char*)d_out;
    (void)hipFuncSetAttribute((const void*)k_qkv, hipFuncAttributeMaxDynamicSharedMemorySize, 163840);
    (void)hipFuncSetAttribute((const void*)k_main, hipFuncAttributeMaxDynamicSharedMemorySize, 163840);
    k_qkv<<<240, 512, 163840, stream>>>(a.x, (const char*)d_in[1], a.ws);
    k_main<<<256, 512, 163840, stream>>>(a);
}
```

```cpp
#include <hip/hip_runtime.h>
#include <hip/hip_bf16.h>
#include <stdint.h>
#include <type_traits>

typedef __attribute__((ext_vector_type(8))) short bf16x8;
typedef __attribute__((ext_vector_type(4))) short bf16x4;
typedef __attribute__((ext_vector_type(16))) float f32x16;
typedef __attribute__((ext_vector_type(4))) float f32x4;
typedef __attribute__((ext_vector_type(2))) float f32x2;
typedef __attribute__((ext_vector_type(4))) unsigned u32x4;
typedef __attribute__((ext_vector_type(2))) unsigned u32x2;
typedef __attribute__((ext_vector_type(2))) __bf16 bfv2;
#define LAS __attribute__((address_space(3)))
#define GAS __attribute__((address_space(1)))
#define DI __device__ __forceinline__

constexpr int S_ = 2048, D_ = 640, H_ = 32, F_ = 1536;
constexpr float EPS = 1e-5f;
constexpr float QSCALE = 0.22360679774997896f * 1.4426950408889634f;

constexpr unsigned SZ_ACT = S_ * D_ * 2;
constexpr int KVT = 2688;
constexpr unsigned WS_FL = 0;
constexpr unsigned FL_RT = WS_FL;
constexpr unsigned FL_AT = FL_RT + 1024;
constexpr unsigned FL_ST = FL_AT + 1024;
constexpr unsigned FL_END = FL_ST + 4 * 4096;
constexpr unsigned WS_Q = 32768;
constexpr unsigned WS_KV = WS_Q + SZ_ACT;
constexpr unsigned WS_O = WS_KV + H_ * 64 * KVT;
constexpr unsigned WS_H = WS_O + SZ_ACT;
constexpr unsigned WS_HS = WS_H + SZ_ACT;
constexpr unsigned WS_X1 = WS_HS + SZ_ACT;
constexpr unsigned WS_G = WS_X1 + SZ_ACT;
constexpr unsigned WS_STH = WS_G + S_ * F_ * 2;
constexpr unsigned WS_STX = WS_STH + S_ * 64;
constexpr unsigned WS_CS = WS_STX + S_ * 64;
constexpr unsigned WS_WO = WS_CS + 24 * 20 * 384;
constexpr unsigned WS_W1 = WS_WO + D_ * D_ * 2;
constexpr unsigned WS_W2 = WS_W1 + D_ * D_ * 2;
constexpr unsigned WS_W3 = WS_W2 + D_ * D_ * 2;
constexpr unsigned WS_W4 = WS_W3 + D_ * F_ * 2;
constexpr unsigned WS_END = WS_W4 + D_ * F_ * 2;

DI unsigned pk2(float a, float b) { bfv2 v = {(__bf16)a, (__bf16)b}; return __builtin_bit_cast(unsigned, v); }
DI float blo(unsigned u) { return __uint_as_float(u << 16); }
DI float bhi(unsigned u) { return __uint_as_float(u & 0xffff0000u); }
DI int rfl(int x) { return __builtin_amdgcn_readfirstlane(x); }
DI float xsum32(float v) { auto r = __builtin_amdgcn_permlane32_swap(__float_as_uint(v), __float_as_uint(v), false, false); return __uint_as_float(r[0]) + __uint_as_float(r[1]); }
DI float xsum16(float v) { auto r = __builtin_amdgcn_permlane16_swap(__float_as_uint(v), __float_as_uint(v), false, false); return __uint_as_float(r[0]) + __uint_as_float(r[1]); }
DI float xmax32(float v) { auto r = __builtin_amdgcn_permlane32_swap(__float_as_uint(v), __float_as_uint(v), false, false); return fmaxf(__uint_as_float(r[0]), __uint_as_float(r[1])); }
DI f32x2 gelu2(f32x2 x) {
    constexpr float c1 = -2.f * 0.7978845608028654f * 1.4426950408889634f, c2 = c1 * 0.044715f;
    const f32x2 t = x * __builtin_elementwise_fma(x * x, (f32x2){c2, c2}, (f32x2){c1, c1});
    const f32x2 d = (f32x2){__builtin_amdgcn_exp2f(t[0]), __builtin_amdgcn_exp2f(t[1])} + 1.f;
    return x * (f32x2){__builtin_amdgcn_rcpf(d[0]), __builtin_amdgcn_rcpf(d[1])};
}
DI float gelu_t(float x) {
    constexpr float c1 = -2.f * 0.7978845608028654f * 1.4426950408889634f, c2 = c1 * 0.044715f;
    const float e = __builtin_amdgcn_exp2f(x * __builtin_fmaf(x * x, c2, c1));
    return x * __builtin_amdgcn_rcpf(1.f + e);
}
template <int OFF> DI void glds(const void* sbase, unsigned voff, unsigned m0v) {
    asm volatile("s_mov_b32 m0, %2\n\ts_nop 0\n\tglobal_load_lds_dwordx4 %0, %1 offset:%3" ::"v"(voff), "s"(sbase), "s"(m0v), "i"(OFF) : "memory");
}
template <int LOFF> DI void glds_at(const void* sbase, unsigned voff, unsigned m0v) {
    asm volatile("s_add_u32 m0, %2, %3\n\ts_nop 0\n\tglobal_load_lds_dwordx4 %0, %1" ::"v"(voff), "s"(sbase), "s"(m0v), "i"(LOFF) : "memory", "scc");
}
template <int OFF> DI void glds_same(const void* sbase, unsigned voff) {
    asm volatile("global_load_lds_dwordx4 %0, %1 offset:%2" ::"v"(voff), "s"(sbase), "i"(OFF) : "memory");
}
template <int OFF> DI u32x4 gld16(const void* sbase, unsigned voff) { u32x4 r; asm volatile("global_load_dwordx4 %0, %1, %2 offset:%3" : "=v"(r) : "v"(voff), "s"(sbase), "i"(OFF) : "memory"); return r; }
template <int OFF> DI void glds_dev(const void* sbase, unsigned voff, unsigned m0v) {
    asm volatile("s_mov_b32 m0, %2\n\ts_nop 0\n\tglobal_load_lds_dwordx4 %0, %1 offset:%3 sc1" ::"v"(voff), "s"(sbase), "s"(m0v), "i"(OFF) : "memory");
}
template <int OFF> DI void glds_dev_same(const void* sbase, unsigned voff) {
    asm volatile("global_load_lds_dwordx4 %0, %1 offset:%2 sc1" ::"v"(voff), "s"(sbase), "i"(OFF) : "memory");
}
template <int OFF> DI u32x2 gld8(const void* sbase, unsigned voff) { u32x2 r; asm volatile("global_load_dwordx2 %0, %1, %2 offset:%3" : "=v"(r) : "v"(voff), "s"(sbase), "i"(OFF) : "memory"); return r; }
template <int N> DI void vmwait() { asm volatile("s_waitcnt vmcnt(%0)" ::"i"(N) : "memory"); }
DI void lgbar() { asm volatile("s_waitcnt lgkmcnt(0)\n\ts_barrier" ::: "memory"); }
#define PIN(x) asm volatile("" : "+v"(x))
#define SBAR() __builtin_amdgcn_sched_barrier(0)
DI bf16x8 lds16(const LAS char* p) { return *(const LAS bf16x8*)p; }
DI bf16x4 lds8(const LAS char* p) { return *(const LAS bf16x4*)p; }
DI bf16x4 ldstr(const LAS char* p) { return __builtin_amdgcn_ds_read_tr16_b64_v4i16((LAS bf16x4*)p); }
DI bf16x8 cat8(bf16x4 a, bf16x4 b) { return (bf16x8){a[0], a[1], a[2], a[3], b[0], b[1], b[2], b[3]}; }
#define MFMA32(a, b, c) __builtin_amdgcn_mfma_f32_32x32x16_bf16((a), (b), (c), 0, 0, 0)
#define MFMA32K8(a, b, c) __builtin_amdgcn_mfma_f32_32x32x8bf16_1k((a), (b), (c), 0, 0, 0)
#define MFMA16(a, b, c) __builtin_amdgcn_mfma_f32_16x16x32_bf16((a), (b), (c), 0, 0, 0)
#define IC(n) std::integral_constant<int, (n)>{}
DI void mma16(f32x4& c, bf16x8 a, u32x4 b) { asm volatile("v_mfma_f32_16x16x32_bf16 %0, %1, %2, %0" : "+v"(c) : "v"(a), "v"(b)); }
DI void mma16z(f32x4& c, bf16x8 a, u32x4 b) { asm volatile("v_mfma_f32_16x16x32_bf16 %0, %1, %2, 0" : "=&v"(c) : "v"(a), "v"(b)); }
#define MMA_DONE5(a) asm volatile("s_nop 7" : "+v"(a[0]), "+v"(a[1]), "+v"(a[2]), "+v"(a[3]), "+v"(a[4]))
#define MMA_DONE6(a) asm volatile("s_nop 7" : "+v"(a[0]), "+v"(a[1]), "+v"(a[2]), "+v"(a[3]), "+v"(a[4]), "+v"(a[5]))
template <int N> DI void lgwait() { __builtin_amdgcn_s_waitcnt(0xC07F | (N << 8)); }
DI const LAS char* opaque(const LAS char* p) { asm volatile("" : "+v"(p)); return p; }
typedef __amdgpu_buffer_rsrc_t rsrc_t;
DI u32x4 ld16d(rsrc_t r, unsigned voff, unsigned soff) { return __builtin_amdgcn_raw_buffer_load_b128(r, voff, soff, 16); }
DI u32x2 ld8d(rsrc_t r, unsigned voff, unsigned soff) { return __builtin_amdgcn_raw_buffer_load_b64(r, voff, soff, 16); }
DI void st16d(rsrc_t r, u32x4 v, unsigned voff, unsigned soff) { __builtin_amdgcn_raw_buffer_store_b128(v, r, voff + soff, 0, 16); }
DI void st8d(rsrc_t r, u32x2 v, unsigned voff, unsigned soff) { __builtin_amdgcn_raw_buffer_store_b64(v, r, voff, soff, 16); }
DI void set_flag(char* ws, unsigned off) { __hip_atomic_store((GAS unsigned*)(ws + off), 1u, __ATOMIC_RELAXED, __HIP_MEMORY_SCOPE_AGENT); }
DI void wait_flags(char* ws, unsigned off) {
    GAS unsigned* p = (GAS unsigned*)(ws + off);
#define POLL() __hip_atomic_load(p, __ATOMIC_RELAXED, __HIP_MEMORY_SCOPE_AGENT)
    unsigned a = POLL(); __builtin_amdgcn_s_sleep(2); SBAR();
    unsigned b = POLL(); __builtin_amdgcn_s_sleep(2); SBAR();
#pragma unroll 1
    for (unsigned spins = 0; spins < (1u << 20); ++spins) {
        const unsigned c = POLL(); SBAR();
        if (__all(a == 1u)) break;
        a = b; b = c;
    }
    __builtin_amdgcn_fence(__ATOMIC_ACQUIRE, "wavefront");
}

#define STAMP_DECL
#define STAMP_AT(kid, n)

#define QDEP 2
DI bf16x8 sel_rows(int lane) { const short one = ((lane & 15) == ((lane >> 4) & 1)) ? (short)0x3F80 : (short)0; return (bf16x8){one, one, one, one, one, one, one, one}; }
struct QFrag { bf16x8 x0, x1; bf16x4 w0a, w0b, w1a, w1b; };

template <bool VSEC>
DI void qkv_compute(char* __restrict__ ws, LAS char* lds, int mp, int nt, int wv, int lane) {
    const int wm = wv >> 1, wn = wv & 1, r32 = lane & 31, h = lane >> 5;
    STAMP_DECL; STAMP_AT(0, 0);
    const int row0 = wm * 64 + r32, q = (lane & 15) >> 2;
    const unsigned xb = row0 * 128 + ((h ^ ((row0 >> 1) & 7)) << 4);
    const unsigned wb = 16384 + (8 * h + q) * 256 + ((((wn << 1) + wm) ^ q) << 6) + ((lane >> 4) & 1) * 32 + (lane & 3) * 8;
    const LAS char* px0 = lds + xb; const LAS char* px1 = lds + (xb ^ 32); const LAS char* px2 = lds + (xb ^ 64); const LAS char* px3 = lds + (xb ^ 96);
    const LAS char* pw = lds + wb; const LAS char* pv = lds + (wb ^ 64);
    const bf16x8 sel = sel_rows(lane);
    f32x16 a00 = {}, a01 = {}, a10 = {}, a11 = {};
    f32x4 cs = {};
    QFrag fa, fb;
#define QMM(A, W, X) A = VSEC ? MFMA32(X, W, A) : MFMA32(W, X, A); SBAR()
#define QSTEP(F, N, PX, KK) do { const bf16x8 w0_ = cat8(F.w0a, F.w0b), w1_ = cat8(F.w1a, F.w1b); \
        QMM(a00, w0_, F.x0); N.w0a = ldstr(pw + (KK) * 4096); N.w0b = ldstr(pw + (KK) * 4096 + 1024); SBAR(); \
        QMM(a01, w0_, F.x1); N.x0 = lds16(PX); N.x1 = lds16(PX + 4096); SBAR(); \
        QMM(a10, w1_, F.x0); N.w1a = ldstr(pv + (KK) * 4096); N.w1b = ldstr(pv + (KK) * 4096 + 1024); SBAR(); \
        QMM(a11, w1_, F.x1); cs = MFMA16(sel, w0_, cs); SBAR(); } while (0)
    lgbar();
    STAMP_AT(0, 1);
    fa.w0a = ldstr(pw); fa.w0b = ldstr(pw + 1024); fa.x0 = lds16(px0); fa.x1 = lds16(px0 + 4096); fa.w1a = ldstr(pv); fa.w1b = ldstr(pv + 1024);
#pragma unroll 1
    for (int c = 0; c < 10; ++c) {
        lgwait<0>(); SBAR();
        QSTEP(fa, fb, px1, 1); QSTEP(fb, fa, px2, 2); QSTEP(fa, fb, px3, 3);
        const int d = c == 4 ? -4 * 32768 : 32768;
        px0 += d; px1 += d; px2 += d; px3 += d; pw += d; pv += d;
        lgbar();
        QSTEP(fb, fa, px0, 0);
    }
    STAMP_AT(0, 2);
    const LAS float* st_mu = (const LAS float*)(lds + 32768); const LAS float* st_rs = st_mu + 128; LAS float* st_cs = (LAS float*)(lds + 32768 + 1024);
    if (lane < 16) { st_cs[wn * 64 + wm * 32 + lane] = cs[0]; st_cs[wn * 64 + wm * 32 + 16 + lane] = cs[1]; }
    lgbar();
    if (!VSEC) {
        const bool isq = nt < 5;
        const int m0_ = mp * 128 + row0;
        const float mu0 = st_mu[row0], rs0 = st_rs[row0], mu1 = st_mu[row0 + 32], rs1 = st_rs[row0 + 32];
        f32x4 c4[2][4];
#pragma unroll
        for (int ni = 0; ni < 2; ++ni)
#pragma unroll
            for (int q4 = 0; q4 < 4; ++q4) c4[ni][q4] = *(const LAS f32x4*)(st_cs + wn * 64 + ((ni ^ wm) << 5) + 8 * q4 + 4 * h);
        const unsigned rstep = isq ? 1280 : KVT, rb = (isq ? WS_Q : WS_KV) + (m0_ >> 5) * rstep, hs = 64 * rstep;
        const unsigned b01 = r32 * 16, b4 = 1024 + r32 * 8;
        const int qg0 = ((isq ? nt : nt - 5) * 128 + wn * 64) / 4 + h;
        auto quad = [&](const f32x16& u, const f32x16& v, int ni, int q4) {
            const int qg = qg0 + ((ni ^ wm) << 3) + 2 * q4, head = (qg * 205) >> 10, qi = qg - head * 5;
            const unsigned off = rb + head * hs + (qi < 4 ? (qi >> 1) * 512 + (qi & 1) * 8 + b01 : b4);
            const f32x4 c = c4[ni][q4];
            *(u32x2*)(ws + off) = (u32x2){pk2((u[4 * q4] - mu0 * c[0]) * rs0, (u[4 * q4 + 1] - mu0 * c[1]) * rs0), pk2((u[4 * q4 + 2] - mu0 * c[2]) * rs0, (u[4 * q4 + 3] - mu0 * c[3]) * rs0)};
            *(u32x2*)(ws + (off + rstep)) = (u32x2){pk2((v[4 * q4] - mu1 * c[0]) * rs1, (v[4 * q4 + 1] - mu1 * c[1]) * rs1), pk2((v[4 * q4 + 2] - mu1 * c[2]) * rs1, (v[4 * q4 + 3] - mu1 * c[3]) * rs1)};
        };
#pragma unroll
        for (int q4 = 0; q4 < 4; ++q4) { quad(a00, a01, 0, q4); quad(a10, a11, 1, q4); }
    } else {
        unsigned cb[2]; float cn[2]; int dd[2];
#pragma unroll
        for (int ni = 0; ni < 2; ++ni) {
            const int cl = wn * 64 + ((ni ^ wm) << 5) + r32, n = (nt - 10) * 128 + cl, head = (n * 3277) >> 16, d = n - head * 20;
            cn[ni] = st_cs[cl]; dd[ni] = d;
            cb[ni] = WS_KV + (head * 64 + mp * 4 + wm * 2) * KVT + 1280 + h * 352 + d * 16;
        }
        f32x4 mu[8], rs[8];
#pragma unroll
        for (int i = 0; i < 8; ++i) { mu[i] = *(const LAS f32x4*)(st_mu + wm * 64 + 8 * i + 4 * h); rs[i] = *(const LAS f32x4*)(st_rs + wm * 64 + 8 * i + 4 * h); }
        auto part = [&](const f32x16& u, const f32x16& v, int mi, int s) {
            u32x4 ou, ov;
#pragma unroll
            for (int qq = 0; qq < 2; ++qq) {
                const int r = 8 * s + 4 * qq, i = 4 * mi + 2 * s + qq;
                ou[2 * qq] = pk2((u[r] - mu[i][0] * cn[0]) * rs[i][0], (u[r + 1] - mu[i][1] * cn[0]) * rs[i][1]);
                ou[2 * qq + 1] = pk2((u[r + 2] - mu[i][2] * cn[0]) * rs[i][2], (u[r + 3] - mu[i][3] * cn[0]) * rs[i][3]);
                ov[2 * qq] = pk2((v[r] - mu[i][0] * cn[1]) * rs[i][0], (v[r + 1] - mu[i][1] * cn[1]) * rs[i][1]);
                ov[2 * qq + 1] = pk2((v[r + 2] - mu[i][2] * cn[1]) * rs[i][2], (v[r + 3] - mu[i][3] * cn[1]) * rs[i][3]);
            }
            *(u32x4*)(ws + (cb[0] + mi * KVT + s * 704)) = ou;
            *(u32x4*)(ws + (cb[1] + mi * KVT + s * 704)) = ov;
        };
        part(a00, a10, 0, 0); part(a00, a10, 0, 1); part(a01, a11, 1, 0); part(a01, a11, 1, 1);
#pragma unroll
        for (int ni = 0; ni < 2; ++ni)
            if (dd[ni] < 2) {
                const unsigned f = dd[ni] == 0 ? 0x3F803F80u : 0u;
#pragma unroll
                for (int i = 0; i < 4; ++i) *(u32x4*)(ws + (cb[ni] + 320 + (i >> 1) * KVT + (i & 1) * 704)) = (u32x4){f, f, f, f};
            }
    }
}

__global__ void __launch_bounds__(512, 2) k_qkv(const char* __restrict__ x, const char* __restrict__ w, char* __restrict__ ws) {
    extern __shared__ __attribute__((aligned(16))) char smem[];
    LAS char* lds = (LAS char*)smem;
    const int lane = threadIdx.x & 63, wv = rfl(threadIdx.x >> 6);
    const int id = (blockIdx.x & 7) * 30 + (blockIdx.x >> 3);
    const int rg = id / 60, rem = id - rg * 60, nt = rem >> 2, mp = rg * 4 + (rem & 3);
    if (wv < 4) {
        if (nt >= 10) qkv_compute<true>(ws, lds, mp, nt, wv, lane);
        else qkv_compute<false>(ws, lds, mp, nt, wv, lane);
    } else {
        const int lw = wv - 4;
        __builtin_amdgcn_s_setprio(2);
        const unsigned lds0 = (unsigned)(uintptr_t)lds + lw * 4096;
        unsigned xo[4], wo[4];
#pragma unroll
        for (int i = 0; i < 4; ++i) {
            xo[i] = (i * 8 + (lane >> 3)) * 1280 + (((lane & 7) ^ (lane >> 4) ^ ((i & 1) << 2)) << 4);
            wo[i] = (i * 4 + (lane >> 4)) * 3840 + (((lane & 15) ^ ((lane >> 4) << 2)) << 4);
        }
        const char* xa = x + (size_t)(mp * 128 + lw * 32) * 1280;
        const char* wa = w + (size_t)(lw * 16) * 3840 + nt * 256;
        const int row = lw * 32 + (lane & 31);
        const unsigned sw = (lane >> 5) ^ ((row >> 1) & 7);
        const LAS char* px0 = lds + row * 128 + (sw << 4); const LAS char* px1 = lds + row * 128 + ((sw ^ 2) << 4);
        const LAS char* px2 = lds + row * 128 + ((sw ^ 4) << 4); const LAS char* px3 = lds + row * 128 + ((sw ^ 6) << 4);
        const bf16x8 sel = sel_rows(lane);
        f32x4 rsum = {};
        float q0 = 0.f, q1 = 0.f;
        unsigned sl = lds0, so = 0;
        auto period = [&](auto LD, auto ST) {
            constexpr bool ld = decltype(LD)::value, st = decltype(ST)::value;
            bf16x8 v[4];
            if constexpr (st) {
                v[0] = lds16(px0 + so); v[1] = lds16(px1 + so); v[2] = lds16(px2 + so); v[3] = lds16(px3 + so);
                so = so == 4 * 32768 ? 0 : so + 32768;
            }
#define QG(i) do { if constexpr (ld) { if ((i) & 1) glds_at<16384 + ((i) >> 1) * 1024>(wa, wo[(i) >> 1], sl); else glds_at<((i) >> 1) * 1024>(xa, xo[(i) >> 1], sl); } SBAR(); } while (0)
#define QS(i) do { if constexpr (st) { rsum = MFMA16(sel, v[i], rsum); const u32x4 u_ = __builtin_bit_cast(u32x4, v[i]); \
                    _Pragma("unroll") for (int j = 0; j < 4; ++j) { const float lo = blo(u_[j]), hi = bhi(u_[j]); q0 = __builtin_fmaf(lo, lo, q0); q1 = __builtin_fmaf(hi, hi, q1); } \
                    asm volatile("" : "+v"(q0), "+v"(q1)); } SBAR(); } while (0)
            QG(0); QG(1); QG(2); QS(0); QG(3); QS(1); QG(4); QS(2); QG(5); QS(3); QG(6); QG(7);
            if constexpr (ld) { xa += 128; wa += 64 * 3840; sl = sl == lds0 + 4 * 32768 ? lds0 : sl + 32768; }
        };
        constexpr int DEP = QDEP;
#pragma unroll 1
        for (int c = 0; c < DEP; ++c) { period(IC(1), IC(0)); if (c == 1) { vmwait<8>(); lgbar(); } }
#pragma unroll 1
        for (int c = 0; c < 10 - DEP; ++c) { period(IC(1), IC(1)); vmwait<8 * (DEP - 1)>(); lgbar(); }
#pragma unroll 1
        for (int c = 10 - DEP; c < 10; ++c) {
            period(IC(0), IC(1));
            if (DEP > 3 && c == 6) vmwait<16>(); else if (DEP > 2 && c == 7) vmwait<8>(); else vmwait<0>();
            lgbar();
        }
        {
            const float sx = __uint_as_float(__builtin_amdgcn_permlane16_swap(__float_as_uint(rsum[0]), __float_as_uint(rsum[1]), false, false)[0]), sxx = xsum32(q0 + q1);
            const float mu = sx * (1.f / D_), var = fmaxf(sxx * (1.f / D_) - mu * mu, 0.f);
            float rs = __builtin_amdgcn_rsqf(var + EPS);
            if (nt < 5) rs *= QSCALE;
            if (lane < 32) { ((LAS float*)(lds + 32768))[row] = mu; ((LAS float*)(lds + 32768))[128 + row] = rs; }
        }
        lgbar();
        if (blockIdx.x < (FL_END + 4095) / 4096) *(u32x4*)(ws + WS_FL + blockIdx.x * 4096 + (threadIdx.x - 256) * 16) = (u32x4){0u, 0u, 0u, 0u};
    }
}

constexpr int AT_SLOT = 16 * KVT;
constexpr int AT_RT = 2 * AT_SLOT;
constexpr int L_C1 = 158 * 1024, L_C3 = L_C1 + 384, L_BAD = L_C3 + 384, L_ST = L_C1 + 1024, L_CP = 150 * 1024;
constexpr int HS_ = 51200;

struct Args { const char* x; const char* wo; const char* w1; const char* w2; const char* w3; const char* w4; char* ws; char* out; };

DI void at_stage(const char* kv, unsigned lane16, unsigned lds0, int c, int wv) {
    const int o = wv * 5120 + (wv < 2 ? wv : 2) * 1024;
    const char* src = kv + c * AT_SLOT + o;
    const unsigned dst = lds0 + ((c & 1) ^ 1) * AT_SLOT + o;
    glds<0>(src, lane16, dst); glds_same<1024>(src, lane16); glds_same<2048>(src, lane16); glds_same<3072>(src, lane16);
    glds<0>(src + 4096, lane16, dst + 4096);
    if (wv < 2) glds_same<1024>(src + 4096, lane16);
}
template <int NT> DI void load_steps(const char* src, unsigned dst, int j0, int first, int step, int n, unsigned lane16) {
#pragma unroll 1
    for (int j = first; j < n; j += step) {
        const char* s = src + (j0 + j) * (NT * 1024);
        const unsigned d = dst + j * (NT * 1024);
        glds_dev<0>(s, lane16, d); glds_dev_same<1024>(s, lane16); glds_dev_same<2048>(s, lane16); glds_dev_same<3072>(s, lane16);
        glds_dev<0>(s + 4096, lane16, d + 4096);
        if (NT == 6) glds_dev_same<1024>(s + 4096, lane16);
    }
}

template <bool MAXED, typename SIDE>
DI void attn_pass(LAS char* lds, unsigned ka, unsigned kb, unsigned va, bf16x8 qa1, bf16x4 qa2, bf16x8 qb1, bf16x4 qb2, float ma, float mb,
                  f32x16& oa, f32x16& ob, SIDE side) {
    f32x16 ca, cb;
#pragma unroll
    for (int r = 0; r < 16; ++r) { ca[r] = MAXED ? -ma : 0.f; cb[r] = MAXED ? -mb : 0.f; }
    bf16x8 k1 = lds16(lds + AT_SLOT + ka); bf16x4 k2 = lds8(lds + AT_SLOT + kb);
    f32x16 sa = MFMA32(k1, qa1, ca), sb;
    sa = MFMA32K8(k2, qa2, sa);
#define EXP4(A, B, S, I) unsigned A = pk2(__builtin_amdgcn_exp2f(S[I]), __builtin_amdgcn_exp2f(S[I + 1])), \
                                  B = pk2(__builtin_amdgcn_exp2f(S[I + 2]), __builtin_amdgcn_exp2f(S[I + 3])); asm volatile("" : "+v"(A), "+v"(B)); SBAR()
#define HALF(SC, SN, K1, K2, Q1, Q2, CI, O) { \
        EXP4(w0, w1, SC, 0);  SN = MFMA32(K1, Q1, CI); SBAR(); \
        EXP4(w2, w3, SC, 4);  SN = MFMA32K8(K2, Q2, SN); SBAR(); \
        EXP4(w4, w5, SC, 8);  O = MFMA32(v0, __builtin_bit_cast(bf16x8, (u32x4){w0, w1, w2, w3}), O); SBAR(); \
        EXP4(w6, w7, SC, 12); O = MFMA32(v1, __builtin_bit_cast(bf16x8, (u32x4){w4, w5, w6, w7}), O); SBAR(); }
#define TILE(t) do { \
        if (((t) & 1) == 0) __builtin_amdgcn_s_setprio(3 - (t) / 2);        \
        const bf16x8 v0 = lds16(pv + (t) * KVT), v1 = lds16(pv + (t) * KVT + 704); \
        bf16x8 n1; bf16x4 n2; \
        if ((t) < 7) { n1 = lds16(pk1 + ((t) + 1) * KVT); n2 = lds8(pk2_ + ((t) + 1) * KVT); } \
        SBAR(); \
        HALF(sa, sb, k1, k2, qb1, qb2, cb, oa) \
        side(c, IC(t)); SBAR(); \
        if ((t) == 7) { n1 = lds16(lds + (AT_SLOT - cur + ka)); n2 = lds8(lds + (AT_SLOT - cur + kb)); SBAR(); }     \
        HALF(sb, sa, n1, n2, qa1, qa2, ca, ob) \
        k1 = n1; k2 = n2; } while (0)
    unsigned cur = AT_SLOT;
#pragma unroll 1
    for (int c = 0; c < 4; ++c) {
        const LAS char* pv = lds + (cur + va); const LAS char* pk1 = lds + (cur + ka); const LAS char* pk2_ = lds + (cur + kb);
        TILE(0); TILE(1); TILE(2); TILE(3); TILE(4); TILE(5); TILE(6); TILE(7);
        cur = AT_SLOT - cur;
    }
    __builtin_amdgcn_s_setprio(0);
}

DI void put_stats(LAS char* lds, rsrc_t R, unsigned src, int row0, int lane) {
    const unsigned vo = (lane >> 4) * 256 + (lane & 15) * 8;
    const u32x2 s0 = ld8d(R, vo, src), s1 = ld8d(R, vo + 128, src);
    float a = __uint_as_float(s0[0]) + __uint_as_float(s1[0]), q = __uint_as_float(s0[1]) + __uint_as_float(s1[1]);
    a = xsum32(xsum16(a)); q = xsum32(xsum16(q));
    const float mu = a * (1.f / D_);
    if (lane < 16) *(LAS f32x2*)(lds + L_ST + (row0 + lane) * 8) = (f32x2){mu, fmaxf(q * (1.f / D_) - mu * mu, 0.f)};
}

__global__ void __launch_bounds__(512, 2) k_main(Args A) {
    extern __shared__ __attribute__((aligned(16))) char smem[];
    LAS char* lds = (LAS char*)smem;
    char* ws = A.ws;
    const unsigned lds0 = (unsigned)(uintptr_t)lds;
    const int tid = threadIdx.x, lane = tid & 63, wv = rfl(tid >> 6);
    const int b = blockIdx.x, xg = b & 7, bi = b >> 3;
    const unsigned lane16 = lane * 16;
    const rsrc_t R = __builtin_amdgcn_make_buffer_rsrc(ws, 0, WS_END, 0x00020000);
    STAMP_DECL; STAMP_AT(1, 0);
    {
        const int r32 = lane & 31, h = lane >> 5;
        const int head = xg * 4 + (bi >> 3), qb = bi & 7, qt = wv & 3, half = wv >> 2;
        const char* kv = ws + WS_KV + (size_t)head * 64 * KVT;
        const int qrow = qb * 256 + qt * 64 + r32;
        const int unit = wv * 256 + b;
        const bool rt = unit < 1184;
        const int mat = unit < 480 ? (unit * 410) >> 16 : unit < 800 ? 3 : 4, rt_nt = mat == 3 ? 6 : 5;
        const int ru = unit - (mat < 4 ? mat * 160 : 800), rt_nb = mat < 4 ? (ru * 3277) >> 16 : (ru * 1366) >> 16, rt_ks = ru - rt_nb * (mat < 4 ? 20 : 48);
        bf16x8 qa1, qb1; bf16x4 qa2, qb2;
        {
            const char* qp = ws + WS_Q + (size_t)(head * 64 + qb * 8 + qt * 2) * 1280;
            u32x4 a = gld16<0>(qp, lane16), a2 = gld16<1280>(qp, lane16);
            u32x2 e = gld8<1024>(qp, r32 * 8), e2 = gld8<2304>(qp, r32 * 8);
            at_stage(kv, lane16, lds0, 0, wv);
            asm volatile("s_waitcnt vmcnt(0)" : "+v"(a), "+v"(e), "+v"(a2), "+v"(e2)::"memory");
            if (tid == 0) *(LAS unsigned*)(lds + L_BAD) = 0u;
            qa1 = __builtin_bit_cast(bf16x8, a); qb1 = __builtin_bit_cast(bf16x8, a2);
            if (h) { e = (u32x2){0u, 0u}; e2 = e; }
            qa2 = __builtin_bit_cast(bf16x4, e); qb2 = __builtin_bit_cast(bf16x4, e2);
        }
        const unsigned ho = half * 8 * KVT;
        const unsigned ka = lane16 + ho, kb = (h ? 1280 + 21 * 16 : r32 * 8 + 1024) + ho, va = 1280 + h * 352 + (r32 < 21 ? r32 : 21) * 16 + ho;
        f32x16 oa = {}, ob = {};
        lgbar();
        STAMP_AT(1, 1);
        STAMP_AT(1, 2);
        auto stream = [&](int c) {
            if (c < 3) at_stage(kv, lane16, lds0, c + 1, wv);
            else load_steps<5>(ws + WS_WO + (bi & 7) * 102400, lds0 + HS_, 0, half * 12 + qt, 4, 12 + half * 8, lane16);
        };
        const int jobs = rt ? 3 : wv == 5 ? 4 : 0;
        attn_pass<false>(lds, ka, kb, va, qa1, qa2, qb1, qb2, 0.f, 0.f, oa, ob, [&](int c, auto tt) {
            constexpr int t = decltype(tt)::value;
            if constexpr (t == 0 || t == 4) { if (half == t / 4) stream(c); }
            if constexpr (t == 7) {
                vmwait<0>(); lgbar();
                STAMP_AT(1, 25 + c);
                if (c == 1 && tid == 0) set_flag(ws, FL_RT + b * 4);
            }
            if constexpr (t == 2) {
                if ((jobs >> c & 1) == 0) {
                } else if (c == 0) {
                    const int pitch = mat == 3 ? 3072 : 1280, rr = (lane * (mat == 3 ? 43 : 52)) >> 9;
                    const unsigned vo = rr < 4 ? rr * pitch + (lane - rr * rt_nt * 2) * 16 : 0;
                    const char* src = (mat == 0 ? A.wo : mat == 1 ? A.w1 : mat == 2 ? A.w2 : mat == 3 ? A.w3 : A.w4) + rt_nb * rt_nt * 32;
#pragma unroll 1
                    for (int i = 0; i < 8; ++i) {
                        const int k = mat ? rt_ks * 32 + i * 4 : rt_ks < 16 ? rt_ks * 40 + (i >> 2) * 20 + ((i >> 1) & 1) * 4 + (i & 1) * 8 : ((rt_ks - 16) * 8 + i) * 20 + 16;
                        glds<0>(src + k * pitch, vo, lds0 + AT_RT + wv * 8192 + i * 1024);
                    }
                } else if (c == 1) {
                    const int g = lane >> 4, p = lane & 3;
                    const unsigned dst = (mat == 0 ? WS_WO : mat == 1 ? WS_W1 : mat == 2 ? WS_W2 : mat == 3 ? WS_W3 : WS_W4) + (rt_nb * (mat == 4 ? 48 : 20) + rt_ks) * rt_nt * 1024 + lane16;
                    const LAS char* st = lds + AT_RT + wv * 8192 + g * 2048 + ((lane & 15) >> 2) * rt_nt * 32;
                    const LAS char* s0 = st + ((mat != 3 && (rt_nb & 1)) ? 32 : 0) + 16 * p;
                    const LAS char* s4 = mat == 3 ? s0 + 128 : st + ((rt_nb & 1) ? 0 : 128) + 8 * p;
                    bf16x8 f[6];
#pragma unroll
                    for (int u = 0; u < 4; ++u) f[u] = cat8(ldstr(s0 + (u >> 1) * 64 + (u & 1) * 8), ldstr(s0 + (u >> 1) * 64 + (u & 1) * 8 + 1024));
                    f[4] = cat8(ldstr(s4), ldstr(s4 + 1024)); f[5] = cat8(ldstr(s4 + 8), ldstr(s4 + 1032));
#pragma unroll
                    for (int u = 0; u < 5; ++u) st16d(R, __builtin_bit_cast(u32x4, f[u]), dst, u * 1024);
                    if (mat == 3) st16d(R, __builtin_bit_cast(u32x4, f[5]), dst, 5120);
                    if (mat == 1 || mat == 3) {
                        const bf16x8 ones = {0x3F80, 0x3F80, 0x3F80, 0x3F80, 0x3F80, 0x3F80, 0x3F80, 0x3F80};
                        f32x4 cs[6];
#pragma unroll
                        for (int u = 0; u < 6; ++u) cs[u] = MFMA16(f[u], ones, (f32x4){});
                        const int u = lane & 15;
                        const f32x4 mine = u == 0 ? cs[0] : u == 1 ? cs[1] : u == 2 ? cs[2] : u == 3 ? cs[3] : u == 4 ? cs[4] : cs[5];
                        if (u < 6) st16d(R, __builtin_bit_cast(u32x4, mine), u * 64 + g * 16, WS_CS + ((mat == 3 ? 8 + rt_nb : rt_nb) * 20 + rt_ks) * 384);
                    }
                } else {
                    for (unsigned spins = 0; spins < (1u << 20); ++spins) {
                        const u32x4 f = ld16d(R, lane16, FL_RT);
                        if (__all((f[0] & f[1] & f[2] & f[3]) == 1u)) break;
                        __builtin_amdgcn_s_sleep(1);
                    }
                }
            }
        });
        STAMP_AT(1, 3);
        for (int attempt = 0;; ++attempt) {
            LAS char* cbuf = lds + qt * 6144 + lane16;
#pragma unroll
            for (int i = 0; i < 3; ++i)
                *(LAS f32x4*)(cbuf + half * 3072 + i * 1024) = half ? (f32x4){oa[4 * i], oa[4 * i + 1], oa[4 * i + 2], oa[4 * i + 3]} : (f32x4){ob[4 * i], ob[4 * i + 1], ob[4 * i + 2], ob[4 * i + 3]};
            lgbar();
            {
                float o[12];
#pragma unroll
                for (int i = 0; i < 3; ++i) {
                    const f32x4 p = *(const LAS f32x4*)(cbuf + (half ^ 1) * 3072 + i * 1024);
#pragma unroll
                    for (int j = 0; j < 4; ++j) o[4 * i + j] = (half ? ob[4 * i + j] : oa[4 * i + j]) + p[j];
                }
                auto lr = __builtin_amdgcn_permlane32_swap(__float_as_uint(o[8]), __float_as_uint(o[8]), false, false);
                const float l = __uint_as_float(lr[1]);
                const float inv = __builtin_amdgcn_rcpf(l);
                const int row = qrow + 32 * half;
                const unsigned ro = (row >> 4) * 20480;
                st16d(R, (u32x4){pk2(o[0] * inv, o[1] * inv), pk2(o[2] * inv, o[3] * inv), pk2(o[4] * inv, o[5] * inv), pk2(o[6] * inv, o[7] * inv)}, ro + (2 * head + h) * 256 + (row & 15) * 16, WS_O);
                if (!h) st8d(R, (u32x2){pk2(o[8] * inv, o[9] * inv), pk2(o[10] * inv, o[11] * inv)}, ro + 16384 + head * 128 + (row & 15) * 8, WS_O);
                const bool ok = l >= 0x1p-60f && l <= 0x1p60f;
                if (!__all(ok) && lane == 0 && attempt == 0) *(LAS unsigned*)(lds + L_BAD) = 1u;
            }
            vmwait<0>(); lgbar();
            if (attempt == 1 || rfl(*(const LAS unsigned*)(lds + L_BAD)) == 0) break;
            float ma = -INFINITY, mb = -INFINITY;
            at_stage(kv, lane16, lds0, 0, wv);
#pragma unroll 1
            for (int c = 0; c < 4; ++c) {
                vmwait<0>(); lgbar();
                if (c < 3) at_stage(kv, lane16, lds0, c + 1, wv);
                const LAS char* sl = lds + ((c & 1) ^ 1) * AT_SLOT;
#pragma unroll 1
                for (int t = 0; t < 8; ++t) {
                    const bf16x8 k1 = lds16(sl + ka + t * KVT); const bf16x4 k2 = lds8(sl + kb + t * KVT);
                    f32x16 s = MFMA32(k1, qa1, (f32x16){}), u = MFMA32(k1, qb1, (f32x16){});
                    s = MFMA32K8(k2, qa2, s); u = MFMA32K8(k2, qb2, u);
#pragma unroll
                    for (int r = 0; r < 16; ++r) { ma = fmaxf(ma, s[r]); mb = fmaxf(mb, u[r]); }
                }
            }
            ma = xmax32(ma); mb = xmax32(mb);
            LAS float* mbuf = (LAS float*)(lds + AT_SLOT) + qt * 128 + r32;
            if (!h) { mbuf[half * 64] = ma; mbuf[half * 64 + 32] = mb; }
            lgbar();
            ma = fmaxf(mbuf[0], mbuf[64]); mb = fmaxf(mbuf[32], mbuf[96]);
            lgbar();
            at_stage(kv, lane16, lds0, 0, wv);
            vmwait<0>(); lgbar();
            oa = (f32x16){}; ob = (f32x16){};
            attn_pass<true>(lds, ka, kb, va, qa1, qa2, qb1, qb2, ma, mb, oa, ob, [&](int c, auto tt) {
                constexpr int t = decltype(tt)::value;
                if constexpr (t == 0 || t == 4) { if (half == t / 4) stream(c); }
                if constexpr (t == 7) { vmwait<0>(); lgbar(); }
            });
        }
        if (tid == 256) set_flag(ws, FL_AT + qb * 128 + head * 4);
    }
    STAMP_AT(1, 4);
    const int nb = bi & 7, panel = xg * 4 + (bi >> 3), nb3 = bi & 15, panel3 = xg * 2 + (bi >> 4);
    if (wv >= 4) {
        const int lw = wv - 4;
        __builtin_amdgcn_s_setprio(2);
        const char* wsl = ws + nb * 102400;
        if (lane < 48) {
            const unsigned src = (lane < 24 ? nb * 7680 + lane * 16 : (8 + nb3) * 7680 + (lane - 24) * 16) + lw * 1920;
            f32x4 s = {};
#pragma unroll
            for (int k = 0; k < 5; ++k) s += __builtin_bit_cast(f32x4, ld16d(R, src + k * 384, WS_CS));
            *(LAS f32x4*)(lds + L_CP + (lw * 48 + lane) * 16) = s;
        }
#pragma unroll 1
        for (int st = 0; st < 3; ++st) {
            if (lw == 0) {
                const unsigned f = st == 0 ? FL_AT + xg * 128 + (lane & 31) * 4
                                 : st == 1 ? FL_ST + panel * 128 + (lane & 7) * 4
                                           : FL_ST + 4096 + ((panel + 31 + ((lane >> 3) & 3) % 3) & 31) * 128 + (lane & 7) * 4;
                wait_flags(ws, f);
            }
            lgbar();
            if (st == 1) put_stats(lds, R, WS_STH + (panel * 4 + lw) * 1024, lw * 16, lane);
            lgbar();
            if (st < 2) {
                const char* w = wsl + (st == 0 ? WS_W1 : WS_W2);
                load_steps<5>(w, lds0 + st * 2 * HS_, 0, lw, 4, 10, lane16);
            } else load_steps<6>(ws + WS_W3 + nb3 * 122880, lds0 + HS_, 0, lw, 4, 8, lane16);
            if (st == 0 && lw == 3 && lane < 48) {
                const LAS f32x4* cp = (const LAS f32x4*)(lds + L_CP) + lane;
                *(LAS f32x4*)(lds + L_C1 + lane * 16) = (cp[0] + cp[48]) + (cp[96] + cp[144]);
            }
            lgbar();
            if (st < 2) load_steps<5>(wsl + (st == 0 ? WS_W1 : WS_W2), lds0 + (1 - st) * HS_, 10, lw, 4, 10, lane16);
            else { load_steps<6>(ws + WS_W3 + nb3 * 122880, lds0 + HS_ + 8 * 6144, 8, lw, 4, 10, lane16); load_steps<6>(ws + WS_W3 + nb3 * 122880, lds0, 18, lw ^ 2, 4, 2, lane16); }
            vmwait<15>(); lgbar();
            if (tid == 256) set_flag(ws, FL_ST + st * 4096 + panel * 128 + nb * 4);
            vmwait<0>();
        }
        const char* w4 = ws + WS_W4 + nb * 245760;
        if (lw == 0) wait_flags(ws, FL_ST + 2 * 4096 + (panel3 * 2 + ((lane >> 3) & 1)) * 128 + (lane & 7) * 4);
        lgbar();
        put_stats(lds, R, WS_STX + (panel3 * 8 + lw * 2) * 1024, lw * 32, lane); put_stats(lds, R, WS_STX + (panel3 * 8 + lw * 2 + 1) * 1024, lw * 32 + 16, lane);
        lgbar();
        load_steps<5>(w4, lds0 + 3 * 5120, 0, lw, 4, 19, lane16);
        lgbar();
        load_steps<5>(w4, lds0 + 22 * 5120, 19, lw, 4, 10, lane16); load_steps<5>(w4, lds0, 29, lw ^ 2, 4, 3, lane16);
        vmwait<15>(); lgbar();
        if (tid == 256) set_flag(ws, FL_ST + 3 * 4096 + panel3 * 128 + nb3 * 4);
        vmwait<0>();
        if (lw == 0) wait_flags(ws, FL_ST + 3 * 4096 + (panel >> 1) * 128 + (lane & 15) * 4);
        lgbar();
        lgbar();
        load_steps<5>(w4, lds0 + 3 * 5120, 32, lw, 4, 8, lane16);
        lgbar();
        load_steps<5>(w4, lds0 + 11 * 5120, 40, lw, 4, 8, lane16);
        vmwait<10>(); lgbar();
        vmwait<0>(); lgbar();
        return;
    }
    const int r = lane & 15, g = lane >> 4;
    const int mt = panel * 4 + wv;
    const int gb = nb * 80 + ((nb & 1) ? 16 : 0), hb = nb * 80 + ((nb & 1) ? 0 : 64);
    const unsigned foff = (gb >> 5) * 1024 + lane16, hoff = ((hb >> 5) * 64 + (((hb & 31) >> 3) + (g >> 1)) * 16 + r) * 16 + (g & 1) * 8;
    f32x4 keep[5];
    {
        const char* xb = A.x + (size_t)(mt * 16 + r) * 1280;
        const u32x4 u = *(const u32x4*)(xb + gb * 2 + g * 16), v = *(const u32x4*)(xb + gb * 2 + 64 + g * 16); const u32x2 w = *(const u32x2*)(xb + hb * 2 + g * 8);
        keep[0] = (f32x4){blo(u[0]), bhi(u[0]), blo(u[1]), bhi(u[1])}; keep[1] = (f32x4){blo(u[2]), bhi(u[2]), blo(u[3]), bhi(u[3])};
        keep[2] = (f32x4){blo(v[0]), bhi(v[0]), blo(v[1]), bhi(v[1])}; keep[3] = (f32x4){blo(v[2]), bhi(v[2]), blo(v[3]), bhi(v[3])};
        keep[4] = (f32x4){blo(w[0]), bhi(w[0]), blo(w[1]), bhi(w[1])};
    }
    float ia = 1.f, ib = 0.f;
#pragma unroll 1
    for (int st = 0; st < 3; ++st) {
        constexpr int PRE = 9;
        const unsigned ab = (st == 0 ? WS_O : st == 1 ? WS_H : WS_HS) + (mt - 1) * 20480;
        unsigned vo[4];
#pragma unroll
        for (int j = 0; j < 4; ++j) {
            const int rr = r + 16 + (st == 2 ? ((j & 1) ? 1 + (j >> 1) : -1 - (j >> 1)) : 0);
            vo[j] = (unsigned)(mt * 16 - 16 + rr) < (unsigned)S_ ? (rr >> 4) * 20480 + (g * 16 + (rr & 15)) * 16 + j * 5120 : 0x80000000u;
        }
        const unsigned vlo = st == 0 ? 20480 + 15360 + g * 256 + r * 8 : vo[3], vhi = vlo + (st == 0 ? 128 : 8);
        const LAS char* b0 = opaque(lds + (st == 0 ? HS_ : st == 1 ? 0 : 2 * HS_) + lane16);
        const LAS char* b1 = opaque(lds + (st == 0 ? 2 * HS_ : st == 1 ? HS_ : 0) + lane16);
        u32x4 a[20];
        bf16x8 bw[4][5];
        f32x4 acc[5];
#pragma unroll
        for (int t = 0; t < 5; ++t) { acc[t] = keep[t] * ia + ib; PIN(acc[t]); }
#pragma unroll
        for (int k = 0; k < 3; ++k)
#pragma unroll
            for (int t = 0; t < 5; ++t) { bw[k][t] = lds16(b0 + (k * 5 + t) * 1024); SBAR(); }
        lgbar();
        STAMP_AT(1, 5 + 4 * st);
        auto ald = [&](int k) {
            const unsigned io = k % 5 < 4 ? (k % 5) * 1024 : 0, so = ab + (k % 5 < 4 ? 0 : 4096);
            if (k < 16) return ld16d(R, vo[k / 5] + io, so);
            const u32x2 lo = ld8d(R, vlo + io, so), hi = ld8d(R, vhi + io, so);
            return (u32x4){lo[0], lo[1], hi[0], hi[1]};
        };
#pragma unroll
        for (int k = 0; k < PRE; ++k) { a[k] = ald(k); SBAR(); }
#pragma unroll
        for (int k = 0; k < 20; ++k) {
            if (k + PRE < 20) a[k + PRE] = ald(k + PRE);
            if (k == 11) lgbar();
            if (k < 18) lgwait<10>(); else if (k == 18) lgwait<5>(); else lgwait<0>();
            SBAR();
#pragma unroll
            for (int t = 0; t < 5; ++t) {
                mma16(acc[t], bw[k & 3][t], a[k]);
                if (k + 3 < 20) bw[(k + 3) & 3][t] = lds16((k + 3 < 10 ? b0 + (k + 3) * 5120 : b1 + (k - 7) * 5120) + t * 1024);
                SBAR();
            }
        }
        const f32x2 mv = *(const LAS f32x2*)(lds + L_ST + (wv * 16 + r) * 8);
        f32x4 c[5];
#pragma unroll
        for (int t = 0; t < 5; ++t) c[t] = *(const LAS f32x4*)(lds + L_C1 + t * 64 + g * 16);
        SBAR();
        MMA_DONE5(acc);
        STAMP_AT(1, 6 + 4 * st);
        unsigned pk[10];
        if (st == 1) {
            ia = __builtin_amdgcn_rsqf(mv[1] + EPS); ib = -mv[0] * ia;
            const float rs = ia * __builtin_amdgcn_rsqf(mv[1] * ia * ia + EPS);
#pragma unroll
            for (int j = 0; j < 10; ++j) {
                const f32x2 u = {acc[j >> 1][(j & 1) * 2], acc[j >> 1][(j & 1) * 2 + 1]}, cc = {c[j >> 1][(j & 1) * 2], c[j >> 1][(j & 1) * 2 + 1]};
                const f32x2 y = gelu2((u - cc * mv[0]) * rs);
                pk[j] = pk2(y[0], y[1]);
            }
        } else {
#pragma unroll
            for (int j = 0; j < 10; ++j) pk[j] = pk2(acc[j >> 1][(j & 1) * 2], acc[j >> 1][(j & 1) * 2 + 1]);
        }
        const unsigned ob = (st == 0 ? WS_H : st == 1 ? WS_HS : WS_X1) + mt * 20480;
        st16d(R, (u32x4){pk[0], pk[1], pk[2], pk[3]}, foff, ob);
        st16d(R, (u32x4){pk[4], pk[5], pk[6], pk[7]}, foff + 1024, ob);
        st8d(R, (u32x2){pk[8], pk[9]}, hoff, ob);
        if (st != 1) {
            f32x4 s1 = acc[0], s2 = acc[0] * acc[0];
#pragma unroll
            for (int t = 1; t < 5; ++t) { s1 += acc[t]; s2 = __builtin_elementwise_fma(acc[t], acc[t], s2); }
            const float p1 = xsum32(xsum16((s1[0] + s1[1]) + (s1[2] + s1[3]))), p2 = xsum32(xsum16((s2[0] + s2[1]) + (s2[2] + s2[3])));
            if (g == 0) st8d(R, (u32x2){__float_as_uint(p1), __float_as_uint(p2)}, r * 8, (st == 0 ? WS_STH : WS_STX) + mt * 1024 + nb * 128);
        }
        STAMP_AT(1, 7 + 4 * st);
        lgbar();
        if (st == 0) {
#pragma unroll
            for (int t = 0; t < 5; ++t) keep[t] = acc[t];
            ia = 0.f;
        } else if (st == 2) {
#pragma unroll
            for (int t = 0; t < 5; ++t) keep[t] += acc[t];
        }
        vmwait<0>(); lgbar();
        STAMP_AT(1, 8 + 4 * st);
    }
    {
        constexpr int RA = 8, PRE = 6;
        const int mt3 = panel3 * 8 + wv * 2;
        const unsigned ab = WS_X1 + mt3 * 20480;
        const LAS char* b0 = opaque(lds + HS_ + lane16);
        const LAS char* b1 = opaque(lds + HS_ + 61440 + lane16);
        const LAS char* b2 = opaque(lds + lane16);
#define ALD(k) ld16d(R, lane16 + ((k) & 3) * 1024, ab + ((k) >> 2) * 4096)
#define ALD3(k) ALD((k) + 20)
        u32x4 a[RA][2];
        bf16x8 bw[3][6];
        f32x4 acc[2][6];
#pragma unroll
        for (int k = 0; k < 2; ++k)
#pragma unroll
            for (int t = 0; t < 6; ++t) { bw[k][t] = lds16(b0 + (k * 6 + t) * 1024); SBAR(); }
        lgbar();
        STAMP_AT(1, 17);
#pragma unroll
        for (int k = 0; k < PRE; ++k) { a[k][0] = ALD(k); a[k][1] = ALD3(k); SBAR(); }
#pragma unroll
        for (int k = 0; k < 20; ++k) {
            if (k + PRE < 20) { a[(k + PRE) % RA][0] = ALD(k + PRE); a[(k + PRE) % RA][1] = ALD3(k + PRE); }
            if (k == 8) lgbar();
            if (k < 19) lgwait<6>(); else lgwait<0>();
            SBAR();
#pragma unroll
            for (int t = 0; t < 6; ++t) {
                if (k == 0) { mma16z(acc[0][t], bw[0][t], a[0][0]); mma16z(acc[1][t], bw[0][t], a[0][1]); }
                else { mma16(acc[0][t], bw[k % 3][t], a[k % RA][0]); mma16(acc[1][t], bw[k % 3][t], a[k % RA][1]); }
                if (k + 2 < 20) bw[(k + 2) % 3][t] = lds16((k + 2 < 10 ? b0 + (k + 2) * 6144 : k + 2 < 18 ? b1 + (k - 8) * 6144 : b2 + (k - 16) * 6144) + t * 1024);
                SBAR();
            }
        }
        f32x4 c3[6];
#pragma unroll
        for (int t = 0; t < 6; ++t) c3[t] = *(const LAS f32x4*)(lds + L_C3 + t * 64 + g * 16);
        const f32x2 mv0 = *(const LAS f32x2*)(lds + L_ST + (wv * 32 + r) * 8), mv1 = *(const LAS f32x2*)(lds + L_ST + (wv * 32 + 16 + r) * 8);
        lgbar();
        MMA_DONE6(acc[0]); MMA_DONE6(acc[1]);
        STAMP_AT(1, 18);
#pragma unroll
        for (int mi = 0; mi < 2; ++mi) {
            const float m3 = mi ? mv1[0] : mv0[0], rs = __builtin_amdgcn_rsqf((mi ? mv1[1] : mv0[1]) + EPS);
#pragma unroll
            for (int p = 0; p < 3; ++p) {
                u32x4 o;
#pragma unroll
                for (int j = 0; j < 4; ++j) {
                    const int t = 2 * p + (j >> 1), e = (j & 1) * 2;
                    const f32x2 y = gelu2(((f32x2){acc[mi][t][e], acc[mi][t][e + 1]} - (f32x2){c3[t][e], c3[t][e + 1]} * m3) * rs);
                    o[j] = pk2(y[0], y[1]);
                }
                st16d(R, o, lane16, WS_G + ((mt3 + mi) * 48 + nb3 * 3 + p) * 1024);
            }
        }
        STAMP_AT(1, 19);
        vmwait<0>(); lgbar();
        STAMP_AT(1, 20);
    }
    {
        constexpr int RA = 12, PRE = 10;
        const unsigned ab = WS_G + mt * 49152;
        const LAS char* bp[3] = {opaque(lds + lane16), opaque(lds + 12 * 5120 + lane16), opaque(lds + 24 * 5120 + lane16)};
#define BLD(k, t) lds16(bp[(((k) + 3) & 31) / 12] + ((((k) + 3) & 31) % 12) * 5120 + (t) * 1024)
        u32x4 a[RA];
        bf16x8 bw[4][5];
#pragma unroll
        for (int t = 0; t < 5; ++t) PIN(keep[t]);
#pragma unroll
        for (int k = 0; k < 3; ++k)
#pragma unroll
            for (int t = 0; t < 5; ++t) { bw[k][t] = BLD(k, t); SBAR(); }
        lgbar();
        STAMP_AT(1, 21);
#pragma unroll
        for (int k = 0; k < PRE; ++k) { a[k] = ALD(k); SBAR(); }
#pragma unroll
        for (int k = 0; k < 48; ++k) {
            if (k + PRE < 48) a[(k + PRE) % RA] = ALD(k + PRE);
            if (k == 5 || k == 13 || k == 29 || k == 37) lgbar();
            if (k < 46) lgwait<10>(); else if (k == 46) lgwait<5>(); else lgwait<0>();
            SBAR();
#pragma unroll
            for (int t = 0; t < 5; ++t) {
                mma16(keep[t], bw[k & 3][t], a[k % RA]);
                if (k + 3 < 48) bw[(k + 3) & 3][t] = BLD(k + 3, t);
                SBAR();
            }
        }
        MMA_DONE5(keep);
        STAMP_AT(1, 22);
        unsigned pk[10];
#pragma unroll
        for (int j = 0; j < 10; ++j) pk[j] = pk2(keep[j >> 1][(j & 1) * 2], keep[j >> 1][(j & 1) * 2 + 1]);
        char* ob = A.out + (size_t)(mt * 16 + r) * 1280;
        *(u32x4*)(ob + gb * 2 + g * 16) = (u32x4){pk[0], pk[1], pk[2], pk[3]};
        *(u32x4*)(ob + gb * 2 + 64 + g * 16) = (u32x4){pk[4], pk[5], pk[6], pk[7]};
        *(u32x2*)(ob + hb * 2 + g * 8) = (u32x2){pk[8], pk[9]};
    }
}

extern "C" void kernel_launch(void* const* d_in, const int* in_sizes, int n_in, void* d_out, int out_size, void* d_ws, size_t ws_size, hipStream_t stream) {
    Args a;
    a.x = (const char*)d_in[0]; a.wo = (const char*)d_in[2]; a.w1 = (const char*)d_in[4]; a.w2 = (const char*)d_in[5];
    a.w3 = (const char*)d_in[6]; a.w4 = (const char*)d_in[7]; a.ws = (char*)d_ws; a.out = (char*)d_out;
    (void)hipFuncSetAttribute((const void*)k_qkv, hipFuncAttributeMaxDynamicSharedMemorySize, 163840);
    (void)hipFuncSetAttribute((const void*)k_main, hipFuncAttributeMaxDynamicSharedMemorySize, 163840);
    k_qkv<<<240, 512, 163840, stream>>>(a.x, (const char*)d_in[1], a.ws);
    k_main<<<256, 512, 163840, stream>>>(a);
}
```

```cpp
#include <hip/hip_runtime.h>
#include <hip/hip_bf16.h>
#include <stdint.h>
#include <type_traits>

typedef __attribute__((ext_vector_type(8))) short bf16x8;
typedef __attribute__((ext_vector_type(4))) short bf16x4;
typedef __attribute__((ext_vector_type(16))) float f32x16;
typedef __attribute__((ext_vector_type(4))) float f32x4;
typedef __attribute__((ext_vector_type(2))) float f32x2;
typedef __attribute__((ext_vector_type(4))) unsigned u32x4;
typedef __attribute__((ext_vector_type(2))) unsigned u32x2;
typedef __attribute__((ext_vector_type(2))) __bf16 bfv2;
#define LAS __attribute__((address_space(3)))
#define GAS __attribute__((address_space(1)))
#define DI __device__ __forceinline__

constexpr int S_ = 2048, D_ = 640, H_ = 32, F_ = 1536;
constexpr float EPS = 1e-5f;
constexpr float QSCALE = 0.22360679774997896f * 1.4426950408889634f;

constexpr unsigned SZ_ACT = S_ * D_ * 2;
constexpr int KVT = 2688;
constexpr unsigned FL_RT = 0;
constexpr unsigned MARK = 0xFFFFFFFFu;
constexpr unsigned WS_Q = 32768;
constexpr unsigned WS_KV = WS_Q + SZ_ACT;
constexpr unsigned WS_O = WS_KV + H_ * 64 * KVT;
constexpr unsigned WS_H = WS_O + SZ_ACT;
constexpr unsigned WS_HS = WS_H + SZ_ACT;
constexpr unsigned WS_X1 = WS_HS + SZ_ACT;
constexpr unsigned WS_G = WS_X1 + SZ_ACT;
constexpr unsigned WS_STH = WS_G + S_ * F_ * 2;
constexpr unsigned WS_STX = WS_STH + S_ * 64;
constexpr unsigned WS_CS = WS_STX + S_ * 64;
constexpr unsigned WS_WO = WS_CS + 24 * 20 * 384;
constexpr unsigned WS_W1 = WS_WO + D_ * D_ * 2;
constexpr unsigned WS_W2 = WS_W1 + D_ * D_ * 2;
constexpr unsigned WS_W3 = WS_W2 + D_ * D_ * 2;
constexpr unsigned WS_W4 = WS_W3 + D_ * F_ * 2;
constexpr unsigned WS_END = WS_W4 + D_ * F_ * 2;

DI unsigned pk2(float a, float b) { bfv2 v = {(__bf16)a, (__bf16)b}; return __builtin_bit_cast(unsigned, v); }
DI float blo(unsigned u) { return __uint_as_float(u << 16); }
DI float bhi(unsigned u) { return __uint_as_float(u & 0xffff0000u); }
DI int rfl(int x) { return __builtin_amdgcn_readfirstlane(x); }
DI float xsum32(float v) { auto r = __builtin_amdgcn_permlane32_swap(__float_as_uint(v), __float_as_uint(v), false, false); return __uint_as_float(r[0]) + __uint_as_float(r[1]); }
DI float xsum16(float v) { auto r = __builtin_amdgcn_permlane16_swap(__float_as_uint(v), __float_as_uint(v), false, false); return __uint_as_float(r[0]) + __uint_as_float(r[1]); }
DI float xmax32(float v) { auto r = __builtin_amdgcn_permlane32_swap(__float_as_uint(v), __float_as_uint(v), false, false); return fmaxf(__uint_as_float(r[0]), __uint_as_float(r[1])); }
DI f32x2 gelu2(f32x2 x) {
    constexpr float c1 = -2.f * 0.7978845608028654f * 1.4426950408889634f, c2 = c1 * 0.044715f;
    const f32x2 t = x * __builtin_elementwise_fma(x * x, (f32x2){c2, c2}, (f32x2){c1, c1});
    const f32x2 d = (f32x2){__builtin_amdgcn_exp2f(t[0]), __builtin_amdgcn_exp2f(t[1])} + 1.f;
    return x * (f32x2){__builtin_amdgcn_rcpf(d[0]), __builtin_amdgcn_rcpf(d[1])};
}
DI float gelu_t(float x) {
    constexpr float c1 = -2.f * 0.7978845608028654f * 1.4426950408889634f, c2 = c1 * 0.044715f;
    const float e = __builtin_amdgcn_exp2f(x * __builtin_fmaf(x * x, c2, c1));
    return x * __builtin_amdgcn_rcpf(1.f + e);
}
template <int OFF> DI void glds(const void* sbase, unsigned voff, unsigned m0v) {
    asm volatile("s_mov_b32 m0, %2\n\ts_nop 0\n\tglobal_load_lds_dwordx4 %0, %1 offset:%3" ::"v"(voff), "s"(sbase), "s"(m0v), "i"(OFF) : "memory");
}
template <int LOFF> DI void glds_at(const void* sbase, unsigned voff, unsigned m0v) {
    asm volatile("s_add_u32 m0, %2, %3\n\ts_nop 0\n\tglobal_load_lds_dwordx4 %0, %1" ::"v"(voff), "s"(sbase), "s"(m0v), "i"(LOFF) : "memory", "scc");
}
template <int OFF> DI void glds_same(const void* sbase, unsigned voff) {
    asm volatile("global_load_lds_dwordx4 %0, %1 offset:%2" ::"v"(voff), "s"(sbase), "i"(OFF) : "memory");
}
template <int OFF> DI u32x4 gld16(const void* sbase, unsigned voff) { u32x4 r; asm volatile("global_load_dwordx4 %0, %1, %2 offset:%3" : "=v"(r) : "v"(voff), "s"(sbase), "i"(OFF) : "memory"); return r; }
template <int OFF> DI void glds_dev(const void* sbase, unsigned voff, unsigned m0v) {
    asm volatile("s_mov_b32 m0, %2\n\ts_nop 0\n\tglobal_load_lds_dwordx4 %0, %1 offset:%3 sc1" ::"v"(voff), "s"(sbase), "s"(m0v), "i"(OFF) : "memory");
}
template <int OFF> DI void glds_dev_same(const void* sbase, unsigned voff) {
    asm volatile("global_load_lds_dwordx4 %0, %1 offset:%2 sc1" ::"v"(voff), "s"(sbase), "i"(OFF) : "memory");
}
template <int OFF> DI u32x2 gld8(const void* sbase, unsigned voff) { u32x2 r; asm volatile("global_load_dwordx2 %0, %1, %2 offset:%3" : "=v"(r) : "v"(voff), "s"(sbase), "i"(OFF) : "memory"); return r; }
template <int N> DI void vmwait() { asm volatile("s_waitcnt vmcnt(%0)" ::"i"(N) : "memory"); }
DI void lgbar() { asm volatile("s_waitcnt lgkmcnt(0)\n\ts_barrier" ::: "memory"); }
#define PIN(x) asm volatile("" : "+v"(x))
#define SBAR() __builtin_amdgcn_sched_barrier(0)
DI bf16x8 lds16(const LAS char* p) { return *(const LAS bf16x8*)p; }
DI bf16x4 lds8(const LAS char* p) { return *(const LAS bf16x4*)p; }
DI bf16x4 ldstr(const LAS char* p) { return __builtin_amdgcn_ds_read_tr16_b64_v4i16((LAS bf16x4*)p); }
DI bf16x8 cat8(bf16x4 a, bf16x4 b) { return (bf16x8){a[0], a[1], a[2], a[3], b[0], b[1], b[2], b[3]}; }
#define MFMA32(a, b, c) __builtin_amdgcn_mfma_f32_32x32x16_bf16((a), (b), (c), 0, 0, 0)
#define MFMA32K8(a, b, c) __builtin_amdgcn_mfma_f32_32x32x8bf16_1k((a), (b), (c), 0, 0, 0)
#define MFMA16(a, b, c) __builtin_amdgcn_mfma_f32_16x16x32_bf16((a), (b), (c), 0, 0, 0)
#define IC(n) std::integral_constant<int, (n)>{}
template <int I, int N, class F> DI void sfor(F&& f) { if constexpr (I < N) { f(IC(I)); sfor<I + 1, N>(f); } }
DI void mma16(f32x4& c, bf16x8 a, u32x4 b) { asm volatile("v_mfma_f32_16x16x32_bf16 %0, %1, %2, %0" : "+v"(c) : "v"(a), "v"(b)); }
DI void mma16z(f32x4& c, bf16x8 a, u32x4 b) { asm volatile("v_mfma_f32_16x16x32_bf16 %0, %1, %2, 0" : "=&v"(c) : "v"(a), "v"(b)); }
#define MMA_DONE5(a) asm volatile("s_nop 7" : "+v"(a[0]), "+v"(a[1]), "+v"(a[2]), "+v"(a[3]), "+v"(a[4]))
#define MMA_DONE6(a) asm volatile("s_nop 7" : "+v"(a[0]), "+v"(a[1]), "+v"(a[2]), "+v"(a[3]), "+v"(a[4]), "+v"(a[5]))
template <int N> DI void lgwait() { __builtin_amdgcn_s_waitcnt(0xC07F | (N << 8)); }
DI const LAS char* opaque(const LAS char* p) { asm volatile("" : "+v"(p)); return p; }
typedef __amdgpu_buffer_rsrc_t rsrc_t;
DI u32x4 ld16d(rsrc_t r, unsigned voff, unsigned soff) { return __builtin_amdgcn_raw_buffer_load_b128(r, voff, soff, 16); }
DI u32x2 ld8d(rsrc_t r, unsigned voff, unsigned soff) { return __builtin_amdgcn_raw_buffer_load_b64(r, voff, soff, 16); }
DI void st16d(rsrc_t r, u32x4 v, unsigned voff, unsigned soff) { __builtin_amdgcn_raw_buffer_store_b128(v, r, voff + soff, 0, 16); }
DI void st8d(rsrc_t r, u32x2 v, unsigned voff, unsigned soff) { __builtin_amdgcn_raw_buffer_store_b64(v, r, voff, soff, 16); }
DI void set_flag(char* ws, unsigned off) { __hip_atomic_store((GAS unsigned*)(ws + off), 1u, __ATOMIC_RELAXED, __HIP_MEMORY_SCOPE_AGENT); }
constexpr unsigned SPINS = 1u << 16;
DI unsigned top2(unsigned m, unsigned a, unsigned b) { return max(max(m, a), b); }
DI unsigned top4(unsigned m, u32x4 a) { return top2(top2(m, a[0], a[1]), a[2], a[3]); }
DI void reread() { asm volatile("" ::: "memory"); }
DI bool marked(unsigned m) { return __builtin_expect(__any(m == MARK), 0); }

#define STAMP_DECL
#define STAMP_AT(kid, n)

#define QDEP 2
DI bf16x8 sel_rows(int lane) { const short one = ((lane & 15) == ((lane >> 4) & 1)) ? (short)0x3F80 : (short)0; return (bf16x8){one, one, one, one, one, one, one, one}; }
struct QFrag { bf16x8 x0, x1; bf16x4 w0a, w0b, w1a, w1b; };

template <bool VSEC>
DI void qkv_compute(char* __restrict__ ws, LAS char* lds, int mp, int nt, int wv, int lane) {
    const int wm = wv >> 1, wn = wv & 1, r32 = lane & 31, h = lane >> 5;
    STAMP_DECL; STAMP_AT(0, 0);
    const int row0 = wm * 64 + r32, q = (lane & 15) >> 2;
    const unsigned xb = row0 * 128 + ((h ^ ((row0 >> 1) & 7)) << 4);
    const unsigned wb = 16384 + (8 * h + q) * 256 + ((((wn << 1) + wm) ^ q) << 6) + ((lane >> 4) & 1) * 32 + (lane & 3) * 8;
    const LAS char* px0 = lds + xb; const LAS char* px1 = lds + (xb ^ 32); const LAS char* px2 = lds + (xb ^ 64); const LAS char* px3 = lds + (xb ^ 96);
    const LAS char* pw = lds + wb; const LAS char* pv = lds + (wb ^ 64);
    const bf16x8 sel = sel_rows(lane);
    f32x16 a00 = {}, a01 = {}, a10 = {}, a11 = {};
    f32x4 cs = {};
    QFrag fa, fb;
#define QMM(A, W, X) A = VSEC ? MFMA32(X, W, A) : MFMA32(W, X, A); SBAR()
#define QSTEP(F, N, PX, KK) do { const bf16x8 w0_ = cat8(F.w0a, F.w0b), w1_ = cat8(F.w1a, F.w1b); \
        QMM(a00, w0_, F.x0); N.w0a = ldstr(pw + (KK) * 4096); N.w0b = ldstr(pw + (KK) * 4096 + 1024); SBAR(); \
        QMM(a01, w0_, F.x1); N.x0 = lds16(PX); N.x1 = lds16(PX + 4096); SBAR(); \
        QMM(a10, w1_, F.x0); N.w1a = ldstr(pv + (KK) * 4096); N.w1b = ldstr(pv + (KK) * 4096 + 1024); SBAR(); \
        QMM(a11, w1_, F.x1); cs = MFMA16(sel, w0_, cs); SBAR(); } while (0)
    lgbar();
    STAMP_AT(0, 1);
    fa.w0a = ldstr(pw); fa.w0b = ldstr(pw + 1024); fa.x0 = lds16(px0); fa.x1 = lds16(px0 + 4096); fa.w1a = ldstr(pv); fa.w1b = ldstr(pv + 1024);
#pragma unroll 1
    for (int c = 0; c < 10; ++c) {
        lgwait<0>(); SBAR();
        QSTEP(fa, fb, px1, 1); QSTEP(fb, fa, px2, 2); QSTEP(fa, fb, px3, 3);
        const int d = c == 4 ? -4 * 32768 : 32768;
        px0 += d; px1 += d; px2 += d; px3 += d; pw += d; pv += d;
        lgbar();
        QSTEP(fb, fa, px0, 0);
    }
    STAMP_AT(0, 2);
    const LAS float* st_mu = (const LAS float*)(lds + 32768); const LAS float* st_rs = st_mu + 128; LAS float* st_cs = (LAS float*)(lds + 32768 + 1024);
    if (lane < 16) { st_cs[wn * 64 + wm * 32 + lane] = cs[0]; st_cs[wn * 64 + wm * 32 + 16 + lane] = cs[1]; }
    lgbar();
    if (!VSEC) {
        const bool isq = nt < 5;
        const int m0_ = mp * 128 + row0;
        const float mu0 = st_mu[row0], rs0 = st_rs[row0], mu1 = st_mu[row0 + 32], rs1 = st_rs[row0 + 32];
        f32x4 c4[2][4];
#pragma unroll
        for (int ni = 0; ni < 2; ++ni)
#pragma unroll
            for (int q4 = 0; q4 < 4; ++q4) c4[ni][q4] = *(const LAS f32x4*)(st_cs + wn * 64 + ((ni ^ wm) << 5) + 8 * q4 + 4 * h);
        const unsigned rstep = isq ? 1280 : KVT, rb = (isq ? WS_Q : WS_KV) + (m0_ >> 5) * rstep, hs = 64 * rstep;
        const unsigned b01 = r32 * 16, b4 = 1024 + r32 * 8;
        const int qg0 = ((isq ? nt : nt - 5) * 128 + wn * 64) / 4 + h;
        auto quad = [&](const f32x16& u, const f32x16& v, int ni, int q4) {
            const int qg = qg0 + ((ni ^ wm) << 3) + 2 * q4, head = (qg * 205) >> 10, qi = qg - head * 5;
            const unsigned off = rb + head * hs + (qi < 4 ? (qi >> 1) * 512 + (qi & 1) * 8 + b01 : b4);
            const f32x4 c = c4[ni][q4];
            *(u32x2*)(ws + off) = (u32x2){pk2((u[4 * q4] - mu0 * c[0]) * rs0, (u[4 * q4 + 1] - mu0 * c[1]) * rs0), pk2((u[4 * q4 + 2] - mu0 * c[2]) * rs0, (u[4 * q4 + 3] - mu0 * c[3]) * rs0)};
            *(u32x2*)(ws + (off + rstep)) = (u32x2){pk2((v[4 * q4] - mu1 * c[0]) * rs1, (v[4 * q4 + 1] - mu1 * c[1]) * rs1), pk2((v[4 * q4 + 2] - mu1 * c[2]) * rs1, (v[4 * q4 + 3] - mu1 * c[3]) * rs1)};
        };
#pragma unroll
        for (int q4 = 0; q4 < 4; ++q4) { quad(a00, a01, 0, q4); quad(a10, a11, 1, q4); }
    } else {
        unsigned cb[2]; float cn[2]; int dd[2];
#pragma unroll
        for (int ni = 0; ni < 2; ++ni) {
            const int cl = wn * 64 + ((ni ^ wm) << 5) + r32, n = (nt - 10) * 128 + cl, head = (n * 3277) >> 16, d = n - head * 20;
            cn[ni] = st_cs[cl]; dd[ni] = d;
            cb[ni] = WS_KV + (head * 64 + mp * 4 + wm * 2) * KVT + 1280 + h * 352 + d * 16;
        }
        f32x4 mu[8], rs[8];
#pragma unroll
        for (int i = 0; i < 8; ++i) { mu[i] = *(const LAS f32x4*)(st_mu + wm * 64 + 8 * i + 4 * h); rs[i] = *(const LAS f32x4*)(st_rs + wm * 64 + 8 * i + 4 * h); }
        auto part = [&](const f32x16& u, const f32x16& v, int mi, int s) {
            u32x4 ou, ov;
#pragma unroll
            for (int qq = 0; qq < 2; ++qq) {
                const int r = 8 * s + 4 * qq, i = 4 * mi + 2 * s + qq;
                ou[2 * qq] = pk2((u[r] - mu[i][0] * cn[0]) * rs[i][0], (u[r + 1] - mu[i][1] * cn[0]) * rs[i][1]);
                ou[2 * qq + 1] = pk2((u[r + 2] - mu[i][2] * cn[0]) * rs[i][2], (u[r + 3] - mu[i][3] * cn[0]) * rs[i][3]);
                ov[2 * qq] = pk2((v[r] - mu[i][0] * cn[1]) * rs[i][0], (v[r + 1] - mu[i][1] * cn[1]) * rs[i][1]);
                ov[2 * qq + 1] = pk2((v[r + 2] - mu[i][2] * cn[1]) * rs[i][2], (v[r + 3] - mu[i][3] * cn[1]) * rs[i][3]);
            }
            *(u32x4*)(ws + (cb[0] + mi * KVT + s * 704)) = ou;
            *(u32x4*)(ws + (cb[1] + mi * KVT + s * 704)) = ov;
        };
        part(a00, a10, 0, 0); part(a00, a10, 0, 1); part(a01, a11, 1, 0); part(a01, a11, 1, 1);
#pragma unroll
        for (int ni = 0; ni < 2; ++ni)
            if (dd[ni] < 2) {
                const unsigned f = dd[ni] == 0 ? 0x3F803F80u : 0u;
#pragma unroll
                for (int i = 0; i < 4; ++i) *(u32x4*)(ws + (cb[ni] + 320 + (i >> 1) * KVT + (i & 1) * 704)) = (u32x4){f, f, f, f};
            }
    }
}

__global__ void __launch_bounds__(512, 2) k_qkv(const char* __restrict__ x, const char* __restrict__ w, char* __restrict__ ws) {
    extern __shared__ __attribute__((aligned(16))) char smem[];
    LAS char* lds = (LAS char*)smem;
    const int lane = threadIdx.x & 63, wv = rfl(threadIdx.x >> 6);
    const int id = (blockIdx.x & 7) * 30 + (blockIdx.x >> 3);
    const int rg = id / 60, rem = id - rg * 60, nt = rem >> 2, mp = rg * 4 + (rem & 3);
    if (wv < 4) {
        if (nt >= 10) qkv_compute<true>(ws, lds, mp, nt, wv, lane);
        else qkv_compute<false>(ws, lds, mp, nt, wv, lane);
    } else {
        const int lw = wv - 4;
        __builtin_amdgcn_s_setprio(2);
        const unsigned lds0 = (unsigned)(uintptr_t)lds + lw * 4096;
        unsigned xo[4], wo[4];
#pragma unroll
        for (int i = 0; i < 4; ++i) {
            xo[i] = (i * 8 + (lane >> 3)) * 1280 + (((lane & 7) ^ (lane >> 4) ^ ((i & 1) << 2)) << 4);
            wo[i] = (i * 4 + (lane >> 4)) * 3840 + (((lane & 15) ^ ((lane >> 4) << 2)) << 4);
        }
        const char* xa = x + (size_t)(mp * 128 + lw * 32) * 1280;
        const char* wa = w + (size_t)(lw * 16) * 3840 + nt * 256;
        const int row = lw * 32 + (lane & 31);
        const unsigned sw = (lane >> 5) ^ ((row >> 1) & 7);
        const LAS char* px0 = lds + row * 128 + (sw << 4); const LAS char* px1 = lds + row * 128 + ((sw ^ 2) << 4);
        const LAS char* px2 = lds + row * 128 + ((sw ^ 4) << 4); const LAS char* px3 = lds + row * 128 + ((sw ^ 6) << 4);
        const bf16x8 sel = sel_rows(lane);
        f32x4 rsum = {};
        float q0 = 0.f, q1 = 0.f;
        unsigned sl = lds0, so = 0;
        auto period = [&](auto LD, auto ST) {
            constexpr bool ld = decltype(LD)::value, st = decltype(ST)::value;
            bf16x8 v[4];
            if constexpr (st) {
                v[0] = lds16(px0 + so); v[1] = lds16(px1 + so); v[2] = lds16(px2 + so); v[3] = lds16(px3 + so);
                so = so == 4 * 32768 ? 0 : so + 32768;
            }
#define QG(i) do { if constexpr (ld) { if ((i) & 1) glds_at<16384 + ((i) >> 1) * 1024>(wa, wo[(i) >> 1], sl); else glds_at<((i) >> 1) * 1024>(xa, xo[(i) >> 1], sl); } SBAR(); } while (0)
#define QS(i) do { if constexpr (st) { rsum = MFMA16(sel, v[i], rsum); const u32x4 u_ = __builtin_bit_cast(u32x4, v[i]); \
                    _Pragma("unroll") for (int j = 0; j < 4; ++j) { const float lo = blo(u_[j]), hi = bhi(u_[j]); q0 = __builtin_fmaf(lo, lo, q0); q1 = __builtin_fmaf(hi, hi, q1); } \
                    asm volatile("" : "+v"(q0), "+v"(q1)); } SBAR(); } while (0)
            QG(0); QG(1); QG(2); QS(0); QG(3); QS(1); QG(4); QS(2); QG(5); QS(3); QG(6); QG(7);
            if constexpr (ld) { xa += 128; wa += 64 * 3840; sl = sl == lds0 + 4 * 32768 ? lds0 : sl + 32768; }
        };
        constexpr int DEP = QDEP;
#pragma unroll 1
        for (int c = 0; c < DEP; ++c) { period(IC(1), IC(0)); if (c == 1) { vmwait<8>(); lgbar(); } }
#pragma unroll 1
        for (int c = 0; c < 10 - DEP; ++c) { period(IC(1), IC(1)); vmwait<8 * (DEP - 1)>(); lgbar(); }
#pragma unroll 1
        for (int c = 10 - DEP; c < 10; ++c) {
            period(IC(0), IC(1));
            if (DEP > 3 && c == 6) vmwait<16>(); else if (DEP > 2 && c == 7) vmwait<8>(); else vmwait<0>();
            lgbar();
        }
        {
            const float sx = __uint_as_float(__builtin_amdgcn_permlane16_swap(__float_as_uint(rsum[0]), __float_as_uint(rsum[1]), false, false)[0]), sxx = xsum32(q0 + q1);
            const float mu = sx * (1.f / D_), var = fmaxf(sxx * (1.f / D_) - mu * mu, 0.f);
            float rs = __builtin_amdgcn_rsqf(var + EPS);
            if (nt < 5) rs *= QSCALE;
            if (lane < 32) { ((LAS float*)(lds + 32768))[row] = mu; ((LAS float*)(lds + 32768))[128 + row] = rs; }
        }
        lgbar();
        if (blockIdx.x == 0 && lw == 0) *(u32x4*)(ws + FL_RT + lane * 16) = (u32x4){0u, 0u, 0u, 0u};
    }
}

constexpr int AT_SLOT = 16 * KVT;
constexpr int AT_RT = 2 * AT_SLOT;
constexpr int L_C1 = 158 * 1024, L_C3 = L_C1 + 384, L_BAD = L_C3 + 384, L_ST = L_C1 + 1024, L_CP = 150 * 1024;
constexpr int HS_ = 51200;

struct Args { const char* x; const char* wo; const char* w1; const char* w2; const char* w3; const char* w4; char* ws; char* out; };

DI void at_stage(const char* kv, unsigned lane16, unsigned lds0, int c, int wv) {
    const int o = wv * 5120 + (wv < 2 ? wv : 2) * 1024;
    const char* src = kv + c * AT_SLOT + o;
    const unsigned dst = lds0 + ((c & 1) ^ 1) * AT_SLOT + o;
    glds<0>(src, lane16, dst); glds_same<1024>(src, lane16); glds_same<2048>(src, lane16); glds_same<3072>(src, lane16);
    glds<0>(src + 4096, lane16, dst + 4096);
    if (wv < 2) glds_same<1024>(src + 4096, lane16);
}
template <int NT> DI void load_steps(const char* src, unsigned dst, int j0, int first, int step, int n, unsigned lane16) {
#pragma unroll 1
    for (int j = first; j < n; j += step) {
        const char* s = src + (j0 + j) * (NT * 1024);
        const unsigned d = dst + j * (NT * 1024);
        glds_dev<0>(s, lane16, d); glds_dev_same<1024>(s, lane16); glds_dev_same<2048>(s, lane16); glds_dev_same<3072>(s, lane16);
        glds_dev<0>(s + 4096, lane16, d + 4096);
        if (NT == 6) glds_dev_same<1024>(s + 4096, lane16);
    }
}

template <bool MAXED, typename SIDE>
DI void attn_pass(LAS char* lds, unsigned ka, unsigned kb, unsigned va, bf16x8 qa1, bf16x4 qa2, bf16x8 qb1, bf16x4 qb2, float ma, float mb,
                  f32x16& oa, f32x16& ob, SIDE side) {
    f32x16 ca, cb;
#pragma unroll
    for (int r = 0; r < 16; ++r) { ca[r] = MAXED ? -ma : 0.f; cb[r] = MAXED ? -mb : 0.f; }
    bf16x8 k1 = lds16(lds + AT_SLOT + ka); bf16x4 k2 = lds8(lds + AT_SLOT + kb);
    f32x16 sa = MFMA32(k1, qa1, ca), sb;
    sa = MFMA32K8(k2, qa2, sa);
#define EXP4(A, B, S, I) unsigned A = pk2(__builtin_amdgcn_exp2f(S[I]), __builtin_amdgcn_exp2f(S[I + 1])), \
                                  B = pk2(__builtin_amdgcn_exp2f(S[I + 2]), __builtin_amdgcn_exp2f(S[I + 3])); asm volatile("" : "+v"(A), "+v"(B)); SBAR()
#define HALF(SC, SN, K1, K2, Q1, Q2, CI, O) { \
        EXP4(w0, w1, SC, 0);  SN = MFMA32(K1, Q1, CI); SBAR(); \
        EXP4(w2, w3, SC, 4);  SN = MFMA32K8(K2, Q2, SN); SBAR(); \
        EXP4(w4, w5, SC, 8);  O = MFMA32(v0, __builtin_bit_cast(bf16x8, (u32x4){w0, w1, w2, w3}), O); SBAR(); \
        EXP4(w6, w7, SC, 12); O = MFMA32(v1, __builtin_bit_cast(bf16x8, (u32x4){w4, w5, w6, w7}), O); SBAR(); }
#define TILE(t) do { \
        if (((t) & 1) == 0) __builtin_amdgcn_s_setprio(3 - (t) / 2);        \
        const bf16x8 v0 = lds16(pv + (t) * KVT), v1 = lds16(pv + (t) * KVT + 704); \
        bf16x8 n1; bf16x4 n2; \
        if ((t) < 7) { n1 = lds16(pk1 + ((t) + 1) * KVT); n2 = lds8(pk2_ + ((t) + 1) * KVT); } \
        SBAR(); \
        HALF(sa, sb, k1, k2, qb1, qb2, cb, oa) \
        side(c, IC(t)); SBAR(); \
        if ((t) == 7) { n1 = lds16(lds + (AT_SLOT - cur + ka)); n2 = lds8(lds + (AT_SLOT - cur + kb)); SBAR(); }     \
        HALF(sb, sa, n1, n2, qa1, qa2, ca, ob) \
        k1 = n1; k2 = n2; } while (0)
    unsigned cur = AT_SLOT;
#pragma unroll 1
    for (int c = 0; c < 4; ++c) {
        const LAS char* pv = lds + (cur + va); const LAS char* pk1 = lds + (cur + ka); const LAS char* pk2_ = lds + (cur + kb);
        TILE(0); TILE(1); TILE(2); TILE(3); TILE(4); TILE(5); TILE(6); TILE(7);
        cur = AT_SLOT - cur;
    }
    __builtin_amdgcn_s_setprio(0);
}

DI void put_stats(LAS char* lds, rsrc_t R, unsigned src, int row0, int lane) {
    const unsigned vo = (lane >> 4) * 256 + (lane & 15) * 8;
    u32x2 s0, s1;
#pragma unroll 1
    for (unsigned spins = 0; spins < SPINS; ++spins) {
        reread();
        s0 = ld8d(R, vo, src); s1 = ld8d(R, vo + 128, src);
        if (!marked(top4(0, (u32x4){s0[0], s0[1], s1[0], s1[1]}))) break;
        __builtin_amdgcn_s_sleep(2);
    }
    float a = __uint_as_float(s0[0]) + __uint_as_float(s1[0]), q = __uint_as_float(s0[1]) + __uint_as_float(s1[1]);
    a = xsum32(xsum16(a)); q = xsum32(xsum16(q));
    const float mu = a * (1.f / D_);
    if (lane < 16) *(LAS f32x2*)(lds + L_ST + (row0 + lane) * 8) = (f32x2){mu, fmaxf(q * (1.f / D_) - mu * mu, 0.f)};
}

__global__ void __launch_bounds__(512, 2) k_main(Args A) {
    extern __shared__ __attribute__((aligned(16))) char smem[];
    LAS char* lds = (LAS char*)smem;
    char* ws = A.ws;
    const unsigned lds0 = (unsigned)(uintptr_t)lds;
    const int tid = threadIdx.x, lane = tid & 63, wv = rfl(tid >> 6);
    const int b = blockIdx.x, xg = b & 7, bi = b >> 3;
    const unsigned lane16 = lane * 16;
    const rsrc_t R = __builtin_amdgcn_make_buffer_rsrc(ws, 0, WS_END, 0x00020000);
    const int nb = bi & 7, panel = xg * 4 + (bi >> 3), nb3 = bi & 15, panel3 = xg * 2 + (bi >> 4);
    const int r = lane & 15, g = lane >> 4, mt = panel * 4 + (wv & 3), mt3 = panel3 * 8 + (wv & 3) * 2;
    const int gb = nb * 80 + ((nb & 1) ? 16 : 0), hb = nb * 80 + ((nb & 1) ? 0 : 64);
    const unsigned foff = (gb >> 5) * 1024 + lane16, hoff = ((hb >> 5) * 64 + (((hb & 31) >> 3) + (g >> 1)) * 16 + r) * 16 + (g & 1) * 8;
    STAMP_DECL; STAMP_AT(1, 0);
    {
        const int r32 = lane & 31, h = lane >> 5;
        const int head = xg * 4 + (bi >> 3), qb = bi & 7, qt = wv & 3, half = wv >> 2;
        const char* kv = ws + WS_KV + (size_t)head * 64 * KVT;
        const int qrow = qb * 256 + qt * 64 + r32;
        const int unit = wv * 256 + b;
        const bool rt = unit < 1184;
        const int mat = unit < 480 ? (unit * 410) >> 16 : unit < 800 ? 3 : 4, rt_nt = mat == 3 ? 6 : 5;
        const int ru = unit - (mat < 4 ? mat * 160 : 800), rt_nb = mat < 4 ? (ru * 3277) >> 16 : (ru * 1366) >> 16, rt_ks = ru - rt_nb * (mat < 4 ? 20 : 48);
        bf16x8 qa1, qb1; bf16x4 qa2, qb2;
        {
            const char* qp = ws + WS_Q + (size_t)(head * 64 + qb * 8 + qt * 2) * 1280;
            u32x4 a = gld16<0>(qp, lane16), a2 = gld16<1280>(qp, lane16);
            u32x2 e = gld8<1024>(qp, r32 * 8), e2 = gld8<2304>(qp, r32 * 8);
            at_stage(kv, lane16, lds0, 0, wv);
            asm volatile("s_waitcnt vmcnt(0)" : "+v"(a), "+v"(e), "+v"(a2), "+v"(e2)::"memory");
            if (tid == 0) *(LAS unsigned*)(lds + L_BAD) = 0u;
            qa1 = __builtin_bit_cast(bf16x8, a); qb1 = __builtin_bit_cast(bf16x8, a2);
            if (h) { e = (u32x2){0u, 0u}; e2 = e; }
            qa2 = __builtin_bit_cast(bf16x4, e); qb2 = __builtin_bit_cast(bf16x4, e2);
        }
        const unsigned ho = half * 8 * KVT;
        const unsigned ka = lane16 + ho, kb = (h ? 1280 + 21 * 16 : r32 * 8 + 1024) + ho, va = 1280 + h * 352 + (r32 < 21 ? r32 : 21) * 16 + ho;
        f32x16 oa = {}, ob = {};
        lgbar();
        STAMP_AT(1, 1);
        STAMP_AT(1, 2);
        auto stream = [&](int c) {
            if (c < 3) at_stage(kv, lane16, lds0, c + 1, wv);
            else load_steps<5>(ws + WS_WO + (bi & 7) * 102400, lds0 + HS_, 0, half * 12 + qt, 4, 12 + half * 8, lane16);
        };
        const unsigned oo = ((qrow + 32 * half) >> 4) * 20480, oo16 = oo + (2 * head + h) * 256 + r * 16, oo8 = oo + 16384 + head * 128 + r * 8;
        const int jobs = 1 | (rt ? 2 : 0) | (wv == 5 ? 4 : 0);
        attn_pass<false>(lds, ka, kb, va, qa1, qa2, qb1, qb2, 0.f, 0.f, oa, ob, [&](int c, auto tt) {
            constexpr int t = decltype(tt)::value;
            if constexpr (t == 0 || t == 4) { if (half == t / 4) stream(c); }
            if constexpr (t == 7) {
                vmwait<0>(); lgbar();
                STAMP_AT(1, 25 + c);
                if (c == 1 && tid == 0) set_flag(ws, FL_RT + b * 4);
            }
            if constexpr (t == 2) {
                if ((jobs >> c & 1) == 0) {
                } else if (c == 0) {
                    const u32x4 mk = {MARK, MARK, MARK, MARK};
                    st16d(R, mk, oo16, WS_O);
                    if (!h) st8d(R, (u32x2){MARK, MARK}, oo8, WS_O);
                    if (half) {
#pragma unroll
                        for (int i = 0; i < 3; ++i) {
                            const unsigned o = WS_H + i * SZ_ACT + mt * 20480;
                            st16d(R, mk, foff, o); st16d(R, mk, foff + 1024, o); st8d(R, (u32x2){MARK, MARK}, hoff, o);
                        }
                        if (lane < 32) st8d(R, (u32x2){MARK, MARK}, (lane >> 4) * (S_ * 64) + r * 8, WS_STH + mt * 1024 + nb * 128);
#pragma unroll
                        for (int i = 0; i < 6; ++i) st16d(R, mk, lane16 + (i % 3) * 1024, WS_G + ((mt3 + i / 3) * 48 + nb3 * 3) * 1024);
                    }
                    if (rt) {
                    const int pitch = mat == 3 ? 3072 : 1280, rr = (lane * (mat == 3 ? 43 : 52)) >> 9;
                    const unsigned vo = rr < 4 ? rr * pitch + (lane - rr * rt_nt * 2) * 16 : 0;
                    const char* src = (mat == 0 ? A.wo : mat == 1 ? A.w1 : mat == 2 ? A.w2 : mat == 3 ? A.w3 : A.w4) + rt_nb * rt_nt * 32;
#pragma unroll 1
                    for (int i = 0; i < 8; ++i) {
                        const int k = mat ? rt_ks * 32 + i * 4 : rt_ks < 16 ? rt_ks * 40 + (i >> 2) * 20 + ((i >> 1) & 1) * 4 + (i & 1) * 8 : ((rt_ks - 16) * 8 + i) * 20 + 16;
                        glds<0>(src + k * pitch, vo, lds0 + AT_RT + wv * 8192 + i * 1024);
                    }
                    }
                } else if (c == 1) {
                    const int p = lane & 3;
                    const unsigned dst = (mat == 0 ? WS_WO : mat == 1 ? WS_W1 : mat == 2 ? WS_W2 : mat == 3 ? WS_W3 : WS_W4) + (rt_nb * (mat == 4 ? 48 : 20) + rt_ks) * rt_nt * 1024 + lane16;
                    const LAS char* st = lds + AT_RT + wv * 8192 + g * 2048 + ((lane & 15) >> 2) * rt_nt * 32;
                    const LAS char* s0 = st + ((mat != 3 && (rt_nb & 1)) ? 32 : 0) + 16 * p;
                    const LAS char* s4 = mat == 3 ? s0 + 128 : st + ((rt_nb & 1) ? 0 : 128) + 8 * p;
                    bf16x8 f[6];
#pragma unroll
                    for (int u = 0; u < 4; ++u) f[u] = cat8(ldstr(s0 + (u >> 1) * 64 + (u & 1) * 8), ldstr(s0 + (u >> 1) * 64 + (u & 1) * 8 + 1024));
                    f[4] = cat8(ldstr(s4), ldstr(s4 + 1024)); f[5] = cat8(ldstr(s4 + 8), ldstr(s4 + 1032));
#pragma unroll
                    for (int u = 0; u < 5; ++u) st16d(R, __builtin_bit_cast(u32x4, f[u]), dst, u * 1024);
                    if (mat == 3) st16d(R, __builtin_bit_cast(u32x4, f[5]), dst, 5120);
                    if (mat == 1 || mat == 3) {
                        const bf16x8 ones = {0x3F80, 0x3F80, 0x3F80, 0x3F80, 0x3F80, 0x3F80, 0x3F80, 0x3F80};
                        f32x4 cs[6];
#pragma unroll
                        for (int u = 0; u < 6; ++u) cs[u] = MFMA16(f[u], ones, (f32x4){});
                        const int u = lane & 15;
                        const f32x4 mine = u == 0 ? cs[0] : u == 1 ? cs[1] : u == 2 ? cs[2] : u == 3 ? cs[3] : u == 4 ? cs[4] : cs[5];
                        if (u < 6) st16d(R, __builtin_bit_cast(u32x4, mine), u * 64 + g * 16, WS_CS + ((mat == 3 ? 8 + rt_nb : rt_nb) * 20 + rt_ks) * 384);
                    }
                } else {
                    for (unsigned spins = 0; spins < SPINS; ++spins) {
                        reread();
                        const u32x4 f = ld16d(R, lane16, FL_RT);
                        if (__all((f[0] & f[1] & f[2] & f[3]) == 1u)) break;
                        __builtin_amdgcn_s_sleep(1);
                    }
                }
            }
        });
        STAMP_AT(1, 3);
        bool done = false;
        for (int attempt = 0;; ++attempt) {
            LAS char* cbuf = lds + qt * 6144 + lane16;
#pragma unroll
            for (int i = 0; i < 3; ++i)
                *(LAS f32x4*)(cbuf + half * 3072 + i * 1024) = half ? (f32x4){oa[4 * i], oa[4 * i + 1], oa[4 * i + 2], oa[4 * i + 3]} : (f32x4){ob[4 * i], ob[4 * i + 1], ob[4 * i + 2], ob[4 * i + 3]};
            lgbar();
            {
                float o[12];
#pragma unroll
                for (int i = 0; i < 3; ++i) {
                    const f32x4 p = *(const LAS f32x4*)(cbuf + (half ^ 1) * 3072 + i * 1024);
#pragma unroll
                    for (int j = 0; j < 4; ++j) o[4 * i + j] = (half ? ob[4 * i + j] : oa[4 * i + j]) + p[j];
                }
                auto lr = __builtin_amdgcn_permlane32_swap(__float_as_uint(o[8]), __float_as_uint(o[8]), false, false);
                const float l = __uint_as_float(lr[1]);
                const bool ok = l >= 0x1p-60f && l <= 0x1p60f;
                if (!done) {
                    if (attempt == 1 || __all(ok)) {
                        const float inv = __builtin_amdgcn_rcpf(l);
                        st16d(R, (u32x4){pk2(o[0] * inv, o[1] * inv), pk2(o[2] * inv, o[3] * inv), pk2(o[4] * inv, o[5] * inv), pk2(o[6] * inv, o[7] * inv)}, oo16, WS_O);
                        if (!h) st8d(R, (u32x2){pk2(o[8] * inv, o[9] * inv), pk2(o[10] * inv, o[11] * inv)}, oo8, WS_O);
                        done = true;
                    } else if (lane == 0) *(LAS unsigned*)(lds + L_BAD) = 1u;
                }
            }
            lgbar();
            if (attempt == 1 || rfl(*(const LAS unsigned*)(lds + L_BAD)) == 0) break;
            float ma = -INFINITY, mb = -INFINITY;
            at_stage(kv, lane16, lds0, 0, wv);
#pragma unroll 1
            for (int c = 0; c < 4; ++c) {
                vmwait<0>(); lgbar();
                if (c < 3) at_stage(kv, lane16, lds0, c + 1, wv);
                const LAS char* sl = lds + ((c & 1) ^ 1) * AT_SLOT;
#pragma unroll 1
                for (int t = 0; t < 8; ++t) {
                    const bf16x8 k1 = lds16(sl + ka + t * KVT); const bf16x4 k2 = lds8(sl + kb + t * KVT);
                    f32x16 s = MFMA32(k1, qa1, (f32x16){}), u = MFMA32(k1, qb1, (f32x16){});
                    s = MFMA32K8(k2, qa2, s); u = MFMA32K8(k2, qb2, u);
#pragma unroll
                    for (int r = 0; r < 16; ++r) { ma = fmaxf(ma, s[r]); mb = fmaxf(mb, u[r]); }
                }
            }
            ma = xmax32(ma); mb = xmax32(mb);
            LAS float* mbuf = (LAS float*)(lds + AT_SLOT) + qt * 128 + r32;
            if (!h) { mbuf[half * 64] = ma; mbuf[half * 64 + 32] = mb; }
            lgbar();
            ma = fmaxf(mbuf[0], mbuf[64]); mb = fmaxf(mbuf[32], mbuf[96]);
            lgbar();
            at_stage(kv, lane16, lds0, 0, wv);
            vmwait<0>(); lgbar();
            oa = (f32x16){}; ob = (f32x16){};
            attn_pass<true>(lds, ka, kb, va, qa1, qa2, qb1, qb2, ma, mb, oa, ob, [&](int c, auto tt) {
                constexpr int t = decltype(tt)::value;
                if constexpr (t == 0 || t == 4) { if (half == t / 4) stream(c); }
                if constexpr (t == 7) { vmwait<0>(); lgbar(); }
            });
        }
    }
    STAMP_AT(1, 4);
    if (wv >= 4) {
        const int lw = wv - 4;
        if (lane < 48) {
            const unsigned src = (lane < 24 ? nb * 7680 + lane * 16 : (8 + nb3) * 7680 + (lane - 24) * 16) + lw * 1920;
            f32x4 s = {};
#pragma unroll
            for (int k = 0; k < 5; ++k) s += __builtin_bit_cast(f32x4, ld16d(R, src + k * 384, WS_CS));
            *(LAS f32x4*)(lds + L_CP + (lw * 48 + lane) * 16) = s;
        }
        const char* w3 = ws + WS_W3 + nb3 * 122880;
        const char* w4 = ws + WS_W4 + nb * 245760;
#pragma unroll 1
        for (int st = 0; st < 3; ++st) {
            if (st == 1) put_stats(lds, R, WS_STH + mt * 1024, lw * 16, lane);
            vmwait<0>(); lgbar();
            const char* w = ws + (st == 0 ? WS_W1 : WS_W2) + nb * 102400;
            if (st < 2) { load_steps<5>(w, lds0 + st * 2 * HS_, 0, lw, 4, 10, lane16); load_steps<5>(w, lds0 + (1 + st) * HS_ % (2 * HS_), 10, lw ^ 2, 4, 6, lane16); }
            else load_steps<6>(w3, lds0 + HS_, 0, lw, 4, 14, lane16);
            if (st == 0 && lw == 3 && lane < 48) {
                const LAS f32x4* cp = (const LAS f32x4*)(lds + L_CP) + lane;
                *(LAS f32x4*)(lds + L_C1 + lane * 16) = (cp[0] + cp[48]) + (cp[96] + cp[144]);
            }
            vmwait<5>(); lgbar();
            if (st < 2) load_steps<5>(w, lds0 + (1 + st) * HS_ % (2 * HS_) + 6 * 5120, 16, lw, 4, 4, lane16);
            else { load_steps<6>(w3, lds0 + HS_ + 14 * 6144, 14, lw, 4, 4, lane16); load_steps<6>(w3, lds0, 18, lw, 4, 2, lane16); }
        }
        put_stats(lds, R, WS_STX + mt3 * 1024, lw * 32, lane); put_stats(lds, R, WS_STX + (mt3 + 1) * 1024, lw * 32 + 16, lane);
        vmwait<0>(); lgbar();
        load_steps<5>(w4, lds0 + 3 * 5120, 0, lw, 4, 19, lane16);
        lgbar();
        load_steps<5>(w4, lds0 + 22 * 5120, 19, lw, 4, 10, lane16); load_steps<5>(w4, lds0, 29, lw ^ 2, 4, 3, lane16);
        vmwait<15>(); lgbar();
        vmwait<0>(); lgbar();
        load_steps<5>(w4, lds0 + 3 * 5120, 32, lw, 4, 12, lane16);
        vmwait<0>(); lgbar();
        load_steps<5>(w4, lds0 + 15 * 5120, 44, lw, 4, 4, lane16);
        vmwait<0>(); lgbar();
        return;
    }
    f32x4 keep[5];
    {
        const char* xb = A.x + (size_t)(mt * 16 + r) * 1280;
        const u32x4 u = *(const u32x4*)(xb + gb * 2 + g * 16), v = *(const u32x4*)(xb + gb * 2 + 64 + g * 16); const u32x2 w = *(const u32x2*)(xb + hb * 2 + g * 8);
        keep[0] = (f32x4){blo(u[0]), bhi(u[0]), blo(u[1]), bhi(u[1])}; keep[1] = (f32x4){blo(u[2]), bhi(u[2]), blo(u[3]), bhi(u[3])};
        keep[2] = (f32x4){blo(v[0]), bhi(v[0]), blo(v[1]), bhi(v[1])}; keep[3] = (f32x4){blo(v[2]), bhi(v[2]), blo(v[3]), bhi(v[3])};
        keep[4] = (f32x4){blo(w[0]), bhi(w[0]), blo(w[1]), bhi(w[1])};
    }
    const int srow = mt * 16 + r + ((nb & 2) ? -(1 + (nb >> 2)) : 1 + (nb >> 2));
    const bool sin = (unsigned)srow < (unsigned)S_;
    const unsigned sro = WS_HS + ((srow & (S_ - 1)) >> 4) * 20480 + ((srow & 15) - r) * 16;
    float ia = 1.f, ib = 0.f;
#define FIRST(LD, N) _Pragma("unroll 1") for (unsigned spins = 0; spins < SPINS; ++spins) { reread(); \
            _Pragma("unroll") for (int k = 0; k < PRE; ++k) { LD(k); SBAR(); } \
            unsigned m = 0; _Pragma("unroll") for (int k = 0; k < 4; ++k) m = N(m, k); \
            if (!marked(m)) break; }
#define AGAIN(LD, N, K0) if (marked(run)) { _Pragma("unroll 1") for (unsigned spins = 0; spins < SPINS; ++spins) { reread(); \
            _Pragma("unroll") for (int k_ = (K0); k_ < (K0) + 4; ++k_) LD(k_); \
            unsigned m = 0; _Pragma("unroll") for (int k_ = (K0); k_ < (K0) + 4; ++k_) m = N(m, k_); \
            if (!marked(m)) break; } }
#pragma unroll 1
    for (int st = 0; st < 3; ++st) {
        constexpr int PRE = 12;
        const unsigned ab = (st == 0 ? WS_O : st == 1 ? WS_H : WS_HS) + mt * 20480;
        const unsigned vlo = st == 0 ? g * 256 + r * 8 : lane16, vhi = vlo + (st == 0 ? 128 : 8);
        const LAS char* b0 = opaque(lds + (st == 0 ? HS_ : st == 1 ? 0 : 2 * HS_) + lane16);
        const LAS char* b1 = opaque(lds + (st == 0 ? 2 * HS_ : st == 1 ? HS_ : 0) + lane16);
        u32x4 a[20];
        bf16x8 bw[4][5];
        f32x4 acc[5];
#pragma unroll
        for (int t = 0; t < 5; ++t) { acc[t] = keep[t] * ia + ib; PIN(acc[t]); }
#pragma unroll
        for (int k = 0; k < 3; ++k)
#pragma unroll
            for (int t = 0; t < 5; ++t) { bw[k][t] = lds16(b0 + (k * 5 + t) * 1024); SBAR(); }
#define ALD(k) ld16d(R, lane16 + ((k) & 3) * 1024, ab + ((k) >> 2) * 4096)
#define LD5(k) do { if ((k) < 16) a[k] = ALD(k); else { const u32x2 lo_ = ld8d(R, vlo + ((k) & 3) * 1024, ab + 16384), hi_ = ld8d(R, vhi + ((k) & 3) * 1024, ab + 16384); \
            a[k] = (u32x4){lo_[0], lo_[1], hi_[0], hi_[1]}; } } while (0)
#define MK5(m, k) top4(m, a[k])
        FIRST(LD5, MK5)
        STAMP_AT(1, 5 + 4 * st);
        unsigned run = 0;
        sfor<0, 20>([&](auto kk) __attribute__((always_inline)) {
            constexpr int k = decltype(kk)::value;
            if constexpr (k + PRE < 20) LD5(k + PRE);
            if (k == 7) lgbar();
            if (k < 18) lgwait<10>(); else if (k == 18) lgwait<5>(); else lgwait<0>();
            SBAR();
#pragma unroll
            for (int t = 0; t < 5; ++t) {
                mma16(acc[t], bw[k & 3][t], a[k]);
                if (k + 3 < 20) bw[(k + 3) & 3][t] = lds16((k + 3 < 10 ? b0 + (k + 3) * 5120 : b1 + (k - 7) * 5120) + t * 1024);
                if (k + 4 < 20 && (t == 1 || t == 3)) { run = top2((k & 3) == 0 && t == 1 ? 0u : run, a[(k + 4) % 20][(t - 1) & 3], a[(k + 4) % 20][t & 3]); PIN(run); }
                SBAR();
            }
            if constexpr ((k & 3) == 3 && k + 1 < 20) AGAIN(LD5, MK5, k + 1)
        });
        const f32x2 mv = *(const LAS f32x2*)(lds + L_ST + (wv * 16 + r) * 8);
        f32x4 c[5];
#pragma unroll
        for (int t = 0; t < 5; ++t) c[t] = *(const LAS f32x4*)(lds + L_C1 + t * 64 + g * 16);
        SBAR();
        MMA_DONE5(acc);
        STAMP_AT(1, 6 + 4 * st);
        unsigned pk[10];
        if (st == 1) {
            ia = __builtin_amdgcn_rsqf(mv[1] + EPS); ib = -mv[0] * ia;
            const float rs = ia * __builtin_amdgcn_rsqf(mv[1] * ia * ia + EPS);
#pragma unroll
            for (int j = 0; j < 10; ++j) {
                const f32x2 u = {acc[j >> 1][(j & 1) * 2], acc[j >> 1][(j & 1) * 2 + 1]}, cc = {c[j >> 1][(j & 1) * 2], c[j >> 1][(j & 1) * 2 + 1]};
                const f32x2 y = gelu2((u - cc * mv[0]) * rs);
                pk[j] = sin ? pk2(y[0], y[1]) : 0u;
            }
        } else {
#pragma unroll
            for (int j = 0; j < 10; ++j) pk[j] = pk2(acc[j >> 1][(j & 1) * 2], acc[j >> 1][(j & 1) * 2 + 1]);
        }
        const unsigned ob = st == 1 ? sro : (st == 0 ? WS_H : WS_X1) + mt * 20480;
        st16d(R, (u32x4){pk[0], pk[1], pk[2], pk[3]}, foff, ob);
        st16d(R, (u32x4){pk[4], pk[5], pk[6], pk[7]}, foff + 1024, ob);
        st8d(R, (u32x2){pk[8], pk[9]}, hoff + ob, 0);
        if (st != 1) {
            f32x4 s1 = acc[0], s2 = acc[0] * acc[0];
#pragma unroll
            for (int t = 1; t < 5; ++t) { s1 += acc[t]; s2 = __builtin_elementwise_fma(acc[t], acc[t], s2); }
            const float p1 = xsum32(xsum16((s1[0] + s1[1]) + (s1[2] + s1[3]))), p2 = xsum32(xsum16((s2[0] + s2[1]) + (s2[2] + s2[3])));
            if (g == 0) st8d(R, (u32x2){__float_as_uint(p1), __float_as_uint(p2)}, r * 8, (st == 0 ? WS_STH : WS_STX) + mt * 1024 + nb * 128);
            const float ka_ = st == 0 ? 0.f : 1.f;
#pragma unroll
            for (int t = 0; t < 5; ++t) keep[t] = keep[t] * ka_ + acc[t];
            ia = 0.f;
        }
        STAMP_AT(1, 7 + 4 * st);
        lgbar();
        STAMP_AT(1, 8 + 4 * st);
    }
    {
        constexpr int RA = 9, PRE = 8;
        const unsigned ab = WS_X1 + mt3 * 20480;
        const LAS char* b0 = opaque(lds + HS_ + lane16);
        const LAS char* b1 = opaque(lds + HS_ + 61440 + lane16);
        const LAS char* b2 = opaque(lds + lane16);
        u32x4 a[RA][2];
        bf16x8 bw[3][6];
        f32x4 acc[2][6];
#pragma unroll
        for (int k = 0; k < 2; ++k)
#pragma unroll
            for (int t = 0; t < 6; ++t) { bw[k][t] = lds16(b0 + (k * 6 + t) * 1024); SBAR(); }
#define LD3(k) do { a[(k) % RA][0] = ALD(k); a[(k) % RA][1] = ALD((k) + 20); } while (0)
#define MK3(m, k) top4(top4(m, a[(k) % RA][0]), a[(k) % RA][1])
        FIRST(LD3, MK3)
        STAMP_AT(1, 17);
        unsigned run = 0;
        sfor<0, 20>([&](auto kk) __attribute__((always_inline)) {
            constexpr int k = decltype(kk)::value;
            if constexpr (k + PRE < 20) LD3(k + PRE);
            if (k == 8) lgbar();
            if (k < 19) lgwait<6>(); else lgwait<0>();
            SBAR();
#pragma unroll
            for (int t = 0; t < 6; ++t) {
                if (k == 0) { mma16z(acc[0][t], bw[0][t], a[0][0]); mma16z(acc[1][t], bw[0][t], a[0][1]); }
                else { mma16(acc[0][t], bw[k % 3][t], a[k % RA][0]); mma16(acc[1][t], bw[k % 3][t], a[k % RA][1]); }
                if (k + 2 < 20) bw[(k + 2) % 3][t] = lds16((k + 2 < 10 ? b0 + (k + 2) * 6144 : k + 2 < 18 ? b1 + (k - 8) * 6144 : b2 + (k - 16) * 6144) + t * 1024);
                if (k + 4 < 20 && t >= 2) { run = top2((k & 3) == 0 && t == 2 ? 0u : run, a[(k + 4) % RA][(t >> 1) & 1][(t & 1) * 2], a[(k + 4) % RA][(t >> 1) & 1][(t & 1) * 2 + 1]); PIN(run); }
                SBAR();
            }
            if constexpr ((k & 3) == 3 && k + 1 < 20) AGAIN(LD3, MK3, k + 1)
        });
        f32x4 c3[6];
#pragma unroll
        for (int t = 0; t < 6; ++t) c3[t] = *(const LAS f32x4*)(lds + L_C3 + t * 64 + g * 16);
        const f32x2 mv0 = *(const LAS f32x2*)(lds + L_ST + (wv * 32 + r) * 8), mv1 = *(const LAS f32x2*)(lds + L_ST + (wv * 32 + 16 + r) * 8);
        lgbar();
        MMA_DONE6(acc[0]); MMA_DONE6(acc[1]);
        STAMP_AT(1, 18);
#pragma unroll
        for (int mi = 0; mi < 2; ++mi) {
            const float m3 = mi ? mv1[0] : mv0[0], rs = __builtin_amdgcn_rsqf((mi ? mv1[1] : mv0[1]) + EPS);
#pragma unroll
            for (int p = 0; p < 3; ++p) {
                u32x4 o;
#pragma unroll
                for (int j = 0; j < 4; ++j) {
                    const int t = 2 * p + (j >> 1), e = (j & 1) * 2;
                    const f32x2 y = gelu2(((f32x2){acc[mi][t][e], acc[mi][t][e + 1]} - (f32x2){c3[t][e], c3[t][e + 1]} * m3) * rs);
                    o[j] = pk2(y[0], y[1]);
                }
                st16d(R, o, lane16, WS_G + ((mt3 + mi) * 48 + nb3 * 3 + p) * 1024);
            }
        }
        STAMP_AT(1, 19);
        lgbar();
        STAMP_AT(1, 20);
    }
    {
        constexpr int RA = 13, PRE = 12;
        const unsigned ab = WS_G + mt * 49152;
        const LAS char* bp[3] = {opaque(lds + lane16), opaque(lds + 12 * 5120 + lane16), opaque(lds + 24 * 5120 + lane16)};
#define BLD(k, t) lds16(bp[(((k) + 3) & 31) / 12] + ((((k) + 3) & 31) % 12) * 5120 + (t) * 1024)
        u32x4 a[RA];
        bf16x8 bw[4][5];
#pragma unroll
        for (int t = 0; t < 5; ++t) PIN(keep[t]);
#pragma unroll
        for (int k = 0; k < 3; ++k)
#pragma unroll
            for (int t = 0; t < 5; ++t) { bw[k][t] = BLD(k, t); SBAR(); }
#define LD4(k) a[(k) % RA] = ALD(k)
#define MK4(m, k) top4(m, a[(k) % RA])
        FIRST(LD4, MK4)
        STAMP_AT(1, 21);
        unsigned run = 0;
        sfor<0, 48>([&](auto kk) __attribute__((always_inline)) {
            constexpr int k = decltype(kk)::value;
            if constexpr (k + PRE < 48) LD4(k + PRE);
            if (k == 9 || k == 29 || k == 41) lgbar();
            if (k < 46) lgwait<10>(); else if (k == 46) lgwait<5>(); else lgwait<0>();
            SBAR();
#pragma unroll
            for (int t = 0; t < 5; ++t) {
                mma16(keep[t], bw[k & 3][t], a[k % RA]);
                if (k + 3 < 48) bw[(k + 3) & 3][t] = BLD(k + 3, t);
                if (k + 4 < 48 && (t == 1 || t == 3)) { run = top2((k & 3) == 0 && t == 1 ? 0u : run, a[(k + 4) % RA][(t - 1) & 3], a[(k + 4) % RA][t & 3]); PIN(run); }
                SBAR();
            }
            if constexpr ((k & 3) == 3 && k + 1 < 48) AGAIN(LD4, MK4, k + 1)
        });
        MMA_DONE5(keep);
        STAMP_AT(1, 22);
        unsigned pk[10];
#pragma unroll
        for (int j = 0; j < 10; ++j) pk[j] = pk2(keep[j >> 1][(j & 1) * 2], keep[j >> 1][(j & 1) * 2 + 1]);
        char* ob = A.out + (size_t)(mt * 16 + r) * 1280;
        *(u32x4*)(ob + gb * 2 + g * 16) = (u32x4){pk[0], pk[1], pk[2], pk[3]};
        *(u32x4*)(ob + gb * 2 + 64 + g * 16) = (u32x4){pk[4], pk[5], pk[6], pk[7]};
        *(u32x2*)(ob + hb * 2 + g * 8) = (u32x2){pk[8], pk[9]};
    }
}

extern "C" void kernel_launch(void* const* d_in, const int* in_sizes, int n_in, void* d_out, int out_size, void* d_ws, size_t ws_size, hipStream_t stream) {
    Args a;
    a.x = (const char*)d_in[0]; a.wo = (const char*)d_in[2]; a.w1 = (const char*)d_in[4]; a.w2 = (const char*)d_in[5];
    a.w3 = (const char*)d_in[6]; a.w4 = (const char*)d_in[7]; a.ws = (char*)d_ws; a.out = (char*)d_out;
    (void)hipFuncSetAttribute((const void*)k_qkv, hipFuncAttributeMaxDynamicSharedMemorySize, 163840);
    (void)hipFuncSetAttribute((const void*)k_main, hipFuncAttributeMaxDynamicSharedMemorySize, 163840);
    k_qkv<<<240, 512, 163840, stream>>>(a.x, (const char*)d_in[1], a.ws);
    k_main<<<256, 512, 163840, stream>>>(a);
}
```

```cpp
#include <hip/hip_runtime.h>
#include <hip/hip_bf16.h>
#include <stdint.h>
#include <type_traits>

typedef __attribute__((ext_vector_type(8))) short bf16x8;
typedef __attribute__((ext_vector_type(4))) short bf16x4;
typedef __attribute__((ext_vector_type(16))) float f32x16;
typedef __attribute__((ext_vector_type(4))) float f32x4;
typedef __attribute__((ext_vector_type(2))) float f32x2;
typedef __attribute__((ext_vector_type(4))) unsigned u32x4;
typedef __attribute__((ext_vector_type(2))) unsigned u32x2;
typedef __attribute__((ext_vector_type(2))) __bf16 bfv2;
#define LAS __attribute__((address_space(3)))
#define GAS __attribute__((address_space(1)))
#define DI __device__ __forceinline__

constexpr int S_ = 2048, D_ = 640, H_ = 32, F_ = 1536;
constexpr float EPS = 1e-5f;
constexpr float QSCALE = 0.22360679774997896f * 1.4426950408889634f;

constexpr unsigned SZ_ACT = S_ * D_ * 2;
constexpr int KVT = 2688;
constexpr unsigned FL_RT = 0;
constexpr unsigned MARK = 0xFFFFFFFFu;
constexpr unsigned WS_Q = 32768;
constexpr unsigned WS_KV = WS_Q + SZ_ACT;
constexpr unsigned WS_O = WS_KV + H_ * 64 * KVT;
constexpr unsigned WS_H = WS_O + SZ_ACT;
constexpr unsigned WS_HS = WS_H + SZ_ACT;
constexpr unsigned WS_X1 = WS_HS + SZ_ACT;
constexpr unsigned WS_G = WS_X1 + SZ_ACT;
constexpr unsigned WS_STH = WS_G + S_ * F_ * 2;
constexpr unsigned WS_STX = WS_STH + S_ * 64;
constexpr unsigned WS_CS = WS_STX + S_ * 64;
constexpr unsigned WS_WO = WS_CS + 24 * 20 * 384;
constexpr unsigned WS_W1 = WS_WO + D_ * D_ * 2;
constexpr unsigned WS_W2 = WS_W1 + D_ * D_ * 2;
constexpr unsigned WS_W3 = WS_W2 + D_ * D_ * 2;
constexpr unsigned WS_W4 = WS_W3 + D_ * F_ * 2;
constexpr unsigned WS_END = WS_W4 + D_ * F_ * 2;

DI unsigned pk2(float a, float b) { bfv2 v = {(__bf16)a, (__bf16)b}; return __builtin_bit_cast(unsigned, v); }
DI float blo(unsigned u) { return __uint_as_float(u << 16); }
DI float bhi(unsigned u) { return __uint_as_float(u & 0xffff0000u); }
DI int rfl(int x) { return __builtin_amdgcn_readfirstlane(x); }
DI float xsum32(float v) { auto r = __builtin_amdgcn_permlane32_swap(__float_as_uint(v), __float_as_uint(v), false, false); return __uint_as_float(r[0]) + __uint_as_float(r[1]); }
DI float xsum16(float v) { auto r = __builtin_amdgcn_permlane16_swap(__float_as_uint(v), __float_as_uint(v), false, false); return __uint_as_float(r[0]) + __uint_as_float(r[1]); }
DI float xmax32(float v) { auto r = __builtin_amdgcn_permlane32_swap(__float_as_uint(v), __float_as_uint(v), false, false); return fmaxf(__uint_as_float(r[0]), __uint_as_float(r[1])); }
struct Gelu {
    float k1, k2, irs;
    DI Gelu(float rs) { constexpr float c1 = -2.f * 0.7978845608028654f * 1.4426950408889634f; k1 = c1 * rs; k2 = k1 * 0.044715f * rs * rs; irs = __builtin_amdgcn_rcpf(rs); }
    DI unsigned operator()(f32x2 v) const {
        const f32x2 t = v * __builtin_elementwise_fma(v * v, (f32x2){k2, k2}, (f32x2){k1, k1});
        const f32x2 d = __builtin_elementwise_fma((f32x2){__builtin_amdgcn_exp2f(t[0]), __builtin_amdgcn_exp2f(t[1])}, (f32x2){irs, irs}, (f32x2){irs, irs});
        const f32x2 y = v * (f32x2){__builtin_amdgcn_rcpf(d[0]), __builtin_amdgcn_rcpf(d[1])};
        return pk2(y[0], y[1]);
    }
};
template <int OFF> DI void glds(const void* sbase, unsigned voff, unsigned m0v) {
    asm volatile("s_mov_b32 m0, %2\n\ts_nop 0\n\tglobal_load_lds_dwordx4 %0, %1 offset:%3" ::"v"(voff), "s"(sbase), "s"(m0v), "i"(OFF) : "memory");
}
template <int LOFF> DI void glds_at(const void* sbase, unsigned voff, unsigned m0v) {
    asm volatile("s_add_u32 m0, %2, %3\n\ts_nop 0\n\tglobal_load_lds_dwordx4 %0, %1" ::"v"(voff), "s"(sbase), "s"(m0v), "i"(LOFF) : "memory", "scc");
}
template <int OFF> DI void glds_same(const void* sbase, unsigned voff) {
    asm volatile("global_load_lds_dwordx4 %0, %1 offset:%2" ::"v"(voff), "s"(sbase), "i"(OFF) : "memory");
}
template <int OFF> DI u32x4 gld16(const void* sbase, unsigned voff) { u32x4 r; asm volatile("global_load_dwordx4 %0, %1, %2 offset:%3" : "=v"(r) : "v"(voff), "s"(sbase), "i"(OFF) : "memory"); return r; }
template <int OFF> DI void glds_dev(const void* sbase, unsigned voff, unsigned m0v) {
    asm volatile("s_mov_b32 m0, %2\n\ts_nop 0\n\tglobal_load_lds_dwordx4 %0, %1 offset:%3 sc1" ::"v"(voff), "s"(sbase), "s"(m0v), "i"(OFF) : "memory");
}
template <int OFF> DI void glds_dev_same(const void* sbase, unsigned voff) {
    asm volatile("global_load_lds_dwordx4 %0, %1 offset:%2 sc1" ::"v"(voff), "s"(sbase), "i"(OFF) : "memory");
}
template <int OFF> DI u32x2 gld8(const void* sbase, unsigned voff) { u32x2 r; asm volatile("global_load_dwordx2 %0, %1, %2 offset:%3" : "=v"(r) : "v"(voff), "s"(sbase), "i"(OFF) : "memory"); return r; }
template <int N> DI void vmwait() { asm volatile("s_waitcnt vmcnt(%0)" ::"i"(N) : "memory"); }
DI void lgbar() { asm volatile("s_waitcnt lgkmcnt(0)\n\ts_barrier" ::: "memory"); }
#define PIN(x) asm volatile("" : "+v"(x))
#define SBAR() __builtin_amdgcn_sched_barrier(0)
DI bf16x8 lds16(const LAS char* p) { return *(const LAS bf16x8*)p; }
DI bf16x4 lds8(const LAS char* p) { return *(const LAS bf16x4*)p; }
DI bf16x4 ldstr(const LAS char* p) { return __builtin_amdgcn_ds_read_tr16_b64_v4i16((LAS bf16x4*)p); }
DI bf16x8 cat8(bf16x4 a, bf16x4 b) { return (bf16x8){a[0], a[1], a[2], a[3], b[0], b[1], b[2], b[3]}; }
#define MFMA32(a, b, c) __builtin_amdgcn_mfma_f32_32x32x16_bf16((a), (b), (c), 0, 0, 0)
#define MFMA32K8(a, b, c) __builtin_amdgcn_mfma_f32_32x32x8bf16_1k((a), (b), (c), 0, 0, 0)
#define MFMA16(a, b, c) __builtin_amdgcn_mfma_f32_16x16x32_bf16((a), (b), (c), 0, 0, 0)
#define IC(n) std::integral_constant<int, (n)>{}
template <int I, int N, class F> DI void sfor(F&& f) { if constexpr (I < N) { f(IC(I)); sfor<I + 1, N>(f); } }
DI void mma16(f32x4& c, bf16x8 a, u32x4 b) { asm volatile("v_mfma_f32_16x16x32_bf16 %0, %1, %2, %0" : "+v"(c) : "v"(a), "v"(b)); }
DI void mma16z(f32x4& c, bf16x8 a, u32x4 b) { asm volatile("v_mfma_f32_16x16x32_bf16 %0, %1, %2, 0" : "=&v"(c) : "v"(a), "v"(b)); }
#define MMA_DONE5(a) asm volatile("s_nop 7" : "+v"(a[0]), "+v"(a[1]), "+v"(a[2]), "+v"(a[3]), "+v"(a[4]))
#define MMA_DONE6(a) asm volatile("s_nop 7" : "+v"(a[0]), "+v"(a[1]), "+v"(a[2]), "+v"(a[3]), "+v"(a[4]), "+v"(a[5]))
template <int N> DI void lgwait() { __builtin_amdgcn_s_waitcnt(0xC07F | (N << 8)); }
template <int V, int N> DI void wait2() { __builtin_amdgcn_s_waitcnt((V & 15) | ((V >> 4) << 14) | 0x70 | (N << 8)); }
constexpr int cmin(int a, int b) { return a < b ? a : b; }
constexpr int cmax(int a, int b) { return a > b ? a : b; }
DI const LAS char* opaque(const LAS char* p) { asm volatile("" : "+v"(p)); return p; }
typedef __amdgpu_buffer_rsrc_t rsrc_t;
DI u32x4 ld16d(rsrc_t r, unsigned voff, unsigned soff) { return __builtin_amdgcn_raw_buffer_load_b128(r, voff, soff, 16); }
DI u32x2 ld8d(rsrc_t r, unsigned voff, unsigned soff) { return __builtin_amdgcn_raw_buffer_load_b64(r, voff, soff, 16); }
DI void st16d(rsrc_t r, u32x4 v, unsigned voff, unsigned soff) { __builtin_amdgcn_raw_buffer_store_b128(v, r, voff + soff, 0, 16); }
DI void st8d(rsrc_t r, u32x2 v, unsigned voff, unsigned soff) { __builtin_amdgcn_raw_buffer_store_b64(v, r, voff, soff, 16); }
DI void set_flag(char* ws, unsigned off) { __hip_atomic_store((GAS unsigned*)(ws + off), 1u, __ATOMIC_RELAXED, __HIP_MEMORY_SCOPE_AGENT); }
constexpr unsigned SPINS = 1u << 16;
DI unsigned top2(unsigned m, unsigned a, unsigned b) { return max(max(m, a), b); }
DI unsigned top4(unsigned m, u32x4 a) { return top2(m, a[0], a[3]); }
DI void reread() { asm volatile("" ::: "memory"); }
DI bool marked(unsigned m) { return __builtin_expect(__any(m == MARK), 0); }

#define STAMP_DECL
#define STAMP_AT(kid, n)

#define QDEP 2
DI bf16x8 sel_rows(int lane) { const short one = ((lane & 15) == ((lane >> 4) & 1)) ? (short)0x3F80 : (short)0; return (bf16x8){one, one, one, one, one, one, one, one}; }
struct QFrag { bf16x8 x0, x1; bf16x4 w0a, w0b, w1a, w1b; };

template <bool VSEC>
DI void qkv_compute(char* __restrict__ ws, LAS char* lds, int mp, int nt, int wv, int lane) {
    const int wm = wv >> 1, wn = wv & 1, r32 = lane & 31, h = lane >> 5;
    STAMP_DECL; STAMP_AT(0, 0);
    const int row0 = wm * 64 + r32, q = (lane & 15) >> 2;
    const unsigned xb = row0 * 128 + ((h ^ ((row0 >> 1) & 7)) << 4);
    const unsigned wb = 16384 + (8 * h + q) * 256 + ((((wn << 1) + wm) ^ q) << 6) + ((lane >> 4) & 1) * 32 + (lane & 3) * 8;
    const LAS char* px0 = lds + xb; const LAS char* px1 = lds + (xb ^ 32); const LAS char* px2 = lds + (xb ^ 64); const LAS char* px3 = lds + (xb ^ 96);
    const LAS char* pw = lds + wb; const LAS char* pv = lds + (wb ^ 64);
    const bf16x8 sel = sel_rows(lane);
    f32x16 a00 = {}, a01 = {}, a10 = {}, a11 = {};
    f32x4 cs = {};
    QFrag fa, fb;
#define QMM(A, W, X) A = VSEC ? MFMA32(X, W, A) : MFMA32(W, X, A); SBAR()
#define QSTEP(F, N, PX, KK) do { const bf16x8 w0_ = cat8(F.w0a, F.w0b), w1_ = cat8(F.w1a, F.w1b); \
        QMM(a00, w0_, F.x0); N.w0a = ldstr(pw + (KK) * 4096); N.w0b = ldstr(pw + (KK) * 4096 + 1024); SBAR(); \
        QMM(a01, w0_, F.x1); N.x0 = lds16(PX); N.x1 = lds16(PX + 4096); SBAR(); \
        QMM(a10, w1_, F.x0); N.w1a = ldstr(pv + (KK) * 4096); N.w1b = ldstr(pv + (KK) * 4096 + 1024); SBAR(); \
        QMM(a11, w1_, F.x1); cs = MFMA16(sel, w0_, cs); SBAR(); } while (0)
    lgbar();
    STAMP_AT(0, 1);
    fa.w0a = ldstr(pw); fa.w0b = ldstr(pw + 1024); fa.x0 = lds16(px0); fa.x1 = lds16(px0 + 4096); fa.w1a = ldstr(pv); fa.w1b = ldstr(pv + 1024);
#pragma unroll 1
    for (int c = 0; c < 10; ++c) {
        lgwait<0>(); SBAR();
        QSTEP(fa, fb, px1, 1); QSTEP(fb, fa, px2, 2); QSTEP(fa, fb, px3, 3);
        const int d = c == 4 ? -4 * 32768 : 32768;
        px0 += d; px1 += d; px2 += d; px3 += d; pw += d; pv += d;
        lgbar();
        QSTEP(fb, fa, px0, 0);
    }
    STAMP_AT(0, 2);
    const LAS float* st_mu = (const LAS float*)(lds + 32768); const LAS float* st_rs = st_mu + 128; LAS float* st_cs = (LAS float*)(lds + 32768 + 1024);
    if (lane < 16) { st_cs[wn * 64 + wm * 32 + lane] = cs[0]; st_cs[wn * 64 + wm * 32 + 16 + lane] = cs[1]; }
    lgbar();
    if (!VSEC) {
        const bool isq = nt < 5;
        const int m0_ = mp * 128 + row0;
        const float mu0 = st_mu[row0], rs0 = st_rs[row0], mu1 = st_mu[row0 + 32], rs1 = st_rs[row0 + 32];
        f32x4 c4[2][4];
#pragma unroll
        for (int ni = 0; ni < 2; ++ni)
#pragma unroll
            for (int q4 = 0; q4 < 4; ++q4) c4[ni][q4] = *(const LAS f32x4*)(st_cs + wn * 64 + ((ni ^ wm) << 5) + 8 * q4 + 4 * h);
        const unsigned rstep = isq ? 1280 : KVT, rb = (isq ? WS_Q : WS_KV) + (m0_ >> 5) * rstep, hs = 64 * rstep;
        const unsigned b01 = r32 * 16, b4 = 1024 + r32 * 8;
        const int qg0 = ((isq ? nt : nt - 5) * 128 + wn * 64) / 4 + h;
        auto quad = [&](const f32x16& u, const f32x16& v, int ni, int q4) {
            const int qg = qg0 + ((ni ^ wm) << 3) + 2 * q4, head = (qg * 205) >> 10, qi = qg - head * 5;
            const unsigned off = rb + head * hs + (qi < 4 ? (qi >> 1) * 512 + (qi & 1) * 8 + b01 : b4);
            const f32x4 c = c4[ni][q4];
            *(u32x2*)(ws + off) = (u32x2){pk2((u[4 * q4] - mu0 * c[0]) * rs0, (u[4 * q4 + 1] - mu0 * c[1]) * rs0), pk2((u[4 * q4 + 2] - mu0 * c[2]) * rs0, (u[4 * q4 + 3] - mu0 * c[3]) * rs0)};
            *(u32x2*)(ws + (off + rstep)) = (u32x2){pk2((v[4 * q4] - mu1 * c[0]) * rs1, (v[4 * q4 + 1] - mu1 * c[1]) * rs1), pk2((v[4 * q4 + 2] - mu1 * c[2]) * rs1, (v[4 * q4 + 3] - mu1 * c[3]) * rs1)};
        };
#pragma unroll
        for (int q4 = 0; q4 < 4; ++q4) { quad(a00, a01, 0, q4); quad(a10, a11, 1, q4); }
    } else {
        unsigned cb[2]; float cn[2]; int dd[2];
#pragma unroll
        for (int ni = 0; ni < 2; ++ni) {
            const int cl = wn * 64 + ((ni ^ wm) << 5) + r32, n = (nt - 10) * 128 + cl, head = (n * 3277) >> 16, d = n - head * 20;
            cn[ni] = st_cs[cl]; dd[ni] = d;
            cb[ni] = WS_KV + (head * 64 + mp * 4 + wm * 2) * KVT + 1280 + h * 352 + d * 16;
        }
        f32x4 mu[8], rs[8];
#pragma unroll
        for (int i = 0; i < 8; ++i) { mu[i] = *(const LAS f32x4*)(st_mu + wm * 64 + 8 * i + 4 * h); rs[i] = *(const LAS f32x4*)(st_rs + wm * 64 + 8 * i + 4 * h); }
        auto part = [&](const f32x16& u, const f32x16& v, int mi, int s) {
            u32x4 ou, ov;
#pragma unroll
            for (int qq = 0; qq < 2; ++qq) {
                const int r = 8 * s + 4 * qq, i = 4 * mi + 2 * s + qq;
                ou[2 * qq] = pk2((u[r] - mu[i][0] * cn[0]) * rs[i][0], (u[r + 1] - mu[i][1] * cn[0]) * rs[i][1]);
                ou[2 * qq + 1] = pk2((u[r + 2] - mu[i][2] * cn[0]) * rs[i][2], (u[r + 3] - mu[i][3] * cn[0]) * rs[i][3]);
                ov[2 * qq] = pk2((v[r] - mu[i][0] * cn[1]) * rs[i][0], (v[r + 1] - mu[i][1] * cn[1]) * rs[i][1]);
                ov[2 * qq + 1] = pk2((v[r + 2] - mu[i][2] * cn[1]) * rs[i][2], (v[r + 3] - mu[i][3] * cn[1]) * rs[i][3]);
            }
            *(u32x4*)(ws + (cb[0] + mi * KVT + s * 704)) = ou;
            *(u32x4*)(ws + (cb[1] + mi * KVT + s * 704)) = ov;
        };
        part(a00, a10, 0, 0); part(a00, a10, 0, 1); part(a01, a11, 1, 0); part(a01, a11, 1, 1);
#pragma unroll
        for (int ni = 0; ni < 2; ++ni)
            if (dd[ni] < 2) {
                const unsigned f = dd[ni] == 0 ? 0x3F803F80u : 0u;
#pragma unroll
                for (int i = 0; i < 4; ++i) *(u32x4*)(ws + (cb[ni] + 320 + (i >> 1) * KVT + (i & 1) * 704)) = (u32x4){f, f, f, f};
            }
    }
}

__global__ void __launch_bounds__(512, 2) k_qkv(const char* __restrict__ x, const char* __restrict__ w, char* __restrict__ ws) {
    extern __shared__ __attribute__((aligned(16))) char smem[];
    LAS char* lds = (LAS char*)smem;
    const int lane = threadIdx.x & 63, wv = rfl(threadIdx.x >> 6);
    const int id = (blockIdx.x & 7) * 30 + (blockIdx.x >> 3);
    const int rg = id / 60, rem = id - rg * 60, nt = rem >> 2, mp = rg * 4 + (rem & 3);
    if (wv < 4) {
        if (nt >= 10) qkv_compute<true>(ws, lds, mp, nt, wv, lane);
        else qkv_compute<false>(ws, lds, mp, nt, wv, lane);
    } else {
        const int lw = wv - 4;
        __builtin_amdgcn_s_setprio(2);
        const unsigned lds0 = (unsigned)(uintptr_t)lds + lw * 4096;
        unsigned xo[4], wo[4];
#pragma unroll
        for (int i = 0; i < 4; ++i) {
            xo[i] = (i * 8 + (lane >> 3)) * 1280 + (((lane & 7) ^ (lane >> 4) ^ ((i & 1) << 2)) << 4);
            wo[i] = (i * 4 + (lane >> 4)) * 3840 + (((lane & 15) ^ ((lane >> 4) << 2)) << 4);
        }
        const char* xa = x + (size_t)(mp * 128 + lw * 32) * 1280;
        const char* wa = w + (size_t)(lw * 16) * 3840 + nt * 256;
        const int row = lw * 32 + (lane & 31);
        const unsigned sw = (lane >> 5) ^ ((row >> 1) & 7);
        const LAS char* px0 = lds + row * 128 + (sw << 4); const LAS char* px1 = lds + row * 128 + ((sw ^ 2) << 4);
        const LAS char* px2 = lds + row * 128 + ((sw ^ 4) << 4); const LAS char* px3 = lds + row * 128 + ((sw ^ 6) << 4);
        const bf16x8 sel = sel_rows(lane);
        f32x4 rsum = {};
        float q0 = 0.f, q1 = 0.f;
        unsigned sl = lds0, so = 0;
        auto period = [&](auto LD, auto ST) {
            constexpr bool ld = decltype(LD)::value, st = decltype(ST)::value;
            bf16x8 v[4];
            if constexpr (st) {
                v[0] = lds16(px0 + so); v[1] = lds16(px1 + so); v[2] = lds16(px2 + so); v[3] = lds16(px3 + so);
                so = so == 4 * 32768 ? 0 : so + 32768;
            }
#define QG(i) do { if constexpr (ld) { if ((i) & 1) glds_at<16384 + ((i) >> 1) * 1024>(wa, wo[(i) >> 1], sl); else glds_at<((i) >> 1) * 1024>(xa, xo[(i) >> 1], sl); } SBAR(); } while (0)
#define QS(i) do { if constexpr (st) { rsum = MFMA16(sel, v[i], rsum); const u32x4 u_ = __builtin_bit_cast(u32x4, v[i]); \
                    _Pragma("unroll") for (int j = 0; j < 4; ++j) { const float lo = blo(u_[j]), hi = bhi(u_[j]); q0 = __builtin_fmaf(lo, lo, q0); q1 = __builtin_fmaf(hi, hi, q1); } \
                    asm volatile("" : "+v"(q0), "+v"(q1)); } SBAR(); } while (0)
            QG(0); QG(1); QG(2); QS(0); QG(3); QS(1); QG(4); QS(2); QG(5); QS(3); QG(6); QG(7);
            if constexpr (ld) { xa += 128; wa += 64 * 3840; sl = sl == lds0 + 4 * 32768 ? lds0 : sl + 32768; }
        };
        constexpr int DEP = QDEP;
#pragma unroll 1
        for (int c = 0; c < DEP; ++c) { period(IC(1), IC(0)); if (c == 1) { vmwait<8>(); lgbar(); } }
#pragma unroll 1
        for (int c = 0; c < 10 - DEP; ++c) { period(IC(1), IC(1)); vmwait<8 * (DEP - 1)>(); lgbar(); }
#pragma unroll 1
        for (int c = 10 - DEP; c < 10; ++c) {
            period(IC(0), IC(1));
            if (DEP > 3 && c == 6) vmwait<16>(); else if (DEP > 2 && c == 7) vmwait<8>(); else vmwait<0>();
            lgbar();
        }
        {
            const float sx = __uint_as_float(__builtin_amdgcn_permlane16_swap(__float_as_uint(rsum[0]), __float_as_uint(rsum[1]), false, false)[0]), sxx = xsum32(q0 + q1);
            const float mu = sx * (1.f / D_), var = fmaxf(sxx * (1.f / D_) - mu * mu, 0.f);
            float rs = __builtin_amdgcn_rsqf(var + EPS);
            if (nt < 5) rs *= QSCALE;
            if (lane < 32) { ((LAS float*)(lds + 32768))[row] = mu; ((LAS float*)(lds + 32768))[128 + row] = rs; }
        }
        lgbar();
        if (blockIdx.x == 0 && lw == 0) *(u32x4*)(ws + FL_RT + lane * 16) = (u32x4){0u, 0u, 0u, 0u};
    }
}

constexpr int AT_SLOT = 16 * KVT;
constexpr int AT_RT = 2 * AT_SLOT;
constexpr int L_C1 = 158 * 1024, L_C3 = L_C1 + 384, L_BAD = L_C3 + 384, L_ST = L_C1 + 1024, L_CP = 150 * 1024;
constexpr int HS_ = 51200;

struct Args { const char* x; const char* wo; const char* w1; const char* w2; const char* w3; const char* w4; char* ws; char* out; };

DI void at_stage(const char* kv, unsigned lane16, unsigned lds0, int c, int wv) {
    const int o = wv * 5120 + (wv < 2 ? wv : 2) * 1024;
    const char* src = kv + c * AT_SLOT + o;
    const unsigned dst = lds0 + ((c & 1) ^ 1) * AT_SLOT + o;
    glds<0>(src, lane16, dst); glds_same<1024>(src, lane16); glds_same<2048>(src, lane16); glds_same<3072>(src, lane16);
    glds<0>(src + 4096, lane16, dst + 4096);
    if (wv < 2) glds_same<1024>(src + 4096, lane16);
}
template <int NT> DI void load_steps(const char* src, unsigned dst, int j0, int first, int step, int n, unsigned lane16) {
#pragma unroll 1
    for (int j = first; j < n; j += step) {
        const char* s = src + (j0 + j) * (NT * 1024);
        const unsigned d = dst + j * (NT * 1024);
        glds_dev<0>(s, lane16, d); glds_dev_same<1024>(s, lane16); glds_dev_same<2048>(s, lane16); glds_dev_same<3072>(s, lane16);
        glds_dev<0>(s + 4096, lane16, d + 4096);
        if (NT == 6) glds_dev_same<1024>(s + 4096, lane16);
    }
}

template <bool MAXED, typename SIDE>
DI void attn_pass(LAS char* lds, unsigned ka, unsigned kb, unsigned va, bf16x8 qa1, bf16x4 qa2, bf16x8 qb1, bf16x4 qb2, float ma, float mb,
                  f32x16& oa, f32x16& ob, SIDE side) {
    f32x16 ca, cb;
#pragma unroll
    for (int r = 0; r < 16; ++r) { ca[r] = MAXED ? -ma : 0.f; cb[r] = MAXED ? -mb : 0.f; }
    bf16x8 k1 = lds16(lds + AT_SLOT + ka); bf16x4 k2 = lds8(lds + AT_SLOT + kb);
    f32x16 sa = MFMA32(k1, qa1, ca), sb;
    sa = MFMA32K8(k2, qa2, sa);
#define EXP4(A, B, S, I) unsigned A = pk2(__builtin_amdgcn_exp2f(S[I]), __builtin_amdgcn_exp2f(S[I + 1])), \
                                  B = pk2(__builtin_amdgcn_exp2f(S[I + 2]), __builtin_amdgcn_exp2f(S[I + 3])); asm volatile("" : "+v"(A), "+v"(B)); SBAR()
#define HALF(SC, SN, K1, K2, Q1, Q2, CI, O) { \
        EXP4(w0, w1, SC, 0);  SN = MFMA32(K1, Q1, CI); SBAR(); \
        EXP4(w2, w3, SC, 4);  SN = MFMA32K8(K2, Q2, SN); SBAR(); \
        EXP4(w4, w5, SC, 8);  O = MFMA32(v0, __builtin_bit_cast(bf16x8, (u32x4){w0, w1, w2, w3}), O); SBAR(); \
        EXP4(w6, w7, SC, 12); O = MFMA32(v1, __builtin_bit_cast(bf16x8, (u32x4){w4, w5, w6, w7}), O); SBAR(); }
#define TILE(t) do { \
        if (((t) & 1) == 0) __builtin_amdgcn_s_setprio(3 - (t) / 2);        \
        const bf16x8 v0 = lds16(pv + (t) * KVT), v1 = lds16(pv + (t) * KVT + 704); \
        bf16x8 n1; bf16x4 n2; \
        if ((t) < 7) { n1 = lds16(pk1 + ((t) + 1) * KVT); n2 = lds8(pk2_ + ((t) + 1) * KVT); } \
        SBAR(); \
        HALF(sa, sb, k1, k2, qb1, qb2, cb, oa) \
        side(c, IC(t)); SBAR(); \
        if ((t) == 7) { n1 = lds16(lds + (AT_SLOT - cur + ka)); n2 = lds8(lds + (AT_SLOT - cur + kb)); SBAR(); }     \
        HALF(sb, sa, n1, n2, qa1, qa2, ca, ob) \
        k1 = n1; k2 = n2; } while (0)
    unsigned cur = AT_SLOT;
#pragma unroll 1
    for (int c = 0; c < 4; ++c) {
        const LAS char* pv = lds + (cur + va); const LAS char* pk1 = lds + (cur + ka); const LAS char* pk2_ = lds + (cur + kb);
        TILE(0); TILE(1); TILE(2); TILE(3); TILE(4); TILE(5); TILE(6); TILE(7);
        cur = AT_SLOT - cur;
    }
    __builtin_amdgcn_s_setprio(0);
}

struct Geo {
    int nb, nb3, mt, mt3, gb, hb; unsigned foff, hoff;
    DI Geo(int b, int wv, int lane) {
        const int xg = b & 7, bi = b >> 3, r = lane & 15, g = lane >> 4;
        nb = bi & 7; nb3 = bi & 15; mt = (xg * 4 + (bi >> 3)) * 4 + (wv & 3); mt3 = (xg * 2 + (bi >> 4)) * 8 + (wv & 3) * 2;
        gb = nb * 80 + ((nb & 1) ? 16 : 0); hb = nb * 80 + ((nb & 1) ? 0 : 64);
        foff = (gb >> 5) * 1024 + lane * 16; hoff = ((hb >> 5) * 64 + (((hb & 31) >> 3) + (g >> 1)) * 16 + r) * 16 + (g & 1) * 8;
    }
};
DI int fresh(int x) { asm volatile("" : "+s"(x)); return x; }
DI void put_stats(LAS char* lds, rsrc_t R, unsigned src, int row0, int lane) {
    const unsigned vo = (lane >> 4) * 256 + (lane & 15) * 8;
    u32x2 s0, s1;
#pragma unroll 1
    for (unsigned spins = 0; spins < SPINS; ++spins) {
        reread();
        s0 = ld8d(R, vo, src); s1 = ld8d(R, vo + 128, src);
        if (!marked(max(s0[0], s1[0]))) break;
        __builtin_amdgcn_s_sleep(2);
    }
    float a = __uint_as_float(s0[0]) + __uint_as_float(s1[0]), q = __uint_as_float(s0[1]) + __uint_as_float(s1[1]);
    a = xsum32(xsum16(a)); q = xsum32(xsum16(q));
    const float mu = a * (1.f / D_);
    if (lane < 16) *(LAS f32x2*)(lds + L_ST + (row0 + lane) * 8) = (f32x2){mu, fmaxf(q * (1.f / D_) - mu * mu, 0.f)};
}

__global__ void __launch_bounds__(512, 2) k_main(Args A) {
    extern __shared__ __attribute__((aligned(16))) char smem[];
    LAS char* lds = (LAS char*)smem;
    char* ws = A.ws;
    const unsigned lds0 = (unsigned)(uintptr_t)lds;
    const int tid = threadIdx.x, lane = tid & 63, wv = rfl(tid >> 6);
    const int b = blockIdx.x, xg = b & 7, bi = b >> 3;
    const unsigned lane16 = lane * 16;
    const rsrc_t R = __builtin_amdgcn_make_buffer_rsrc(ws, 0, WS_END, 0x00020000);
    STAMP_DECL; STAMP_AT(1, 0);
    {
        const int r32 = lane & 31, h = lane >> 5;
        const int head = xg * 4 + (bi >> 3), qb = bi & 7, qt = wv & 3, half = wv >> 2;
        const char* kv = ws + WS_KV + (size_t)head * 64 * KVT;
        u32x4 qa, qa_; u32x2 qe, qe_;
        {
            const char* qp = ws + WS_Q + (size_t)(head * 64 + qb * 8 + qt * 2) * 1280;
            qa = gld16<0>(qp, lane16); qa_ = gld16<1280>(qp, lane16); qe = gld8<1024>(qp, r32 * 8); qe_ = gld8<2304>(qp, r32 * 8);
        }
        at_stage(kv, lane16, lds0, 0, wv);
        const int r = lane & 15, g = lane >> 4;
        const unsigned ho = half * 8 * KVT;
        unsigned ka = lane16 + ho, kb = (h ? 1280 + 21 * 16 : r32 * 8 + 1024) + ho, va = 1280 + h * 352 + (r32 < 21 ? r32 : 21) * 16 + ho;
        const int jobs = (wv * 256 + b < 1184 ? 3 : 0) | (wv == 5 ? 4 : 0);
        f32x16 oa = {}, ob = {};
        if (tid == 0) *(LAS unsigned*)(lds + L_BAD) = 0u;
        asm volatile("" : "+v"(ka), "+v"(kb), "+v"(va), "+v"(oa), "+v"(ob));
        asm volatile("s_waitcnt vmcnt(0)" : "+v"(qa), "+v"(qe), "+v"(qa_), "+v"(qe_)::"memory");
        const bf16x8 qa1 = __builtin_bit_cast(bf16x8, qa), qb1 = __builtin_bit_cast(bf16x8, qa_);
        const bf16x4 qa2 = __builtin_bit_cast(bf16x4, h ? (u32x2){0u, 0u} : qe), qb2 = __builtin_bit_cast(bf16x4, h ? (u32x2){0u, 0u} : qe_);
        lgbar();
        STAMP_AT(1, 1);
        STAMP_AT(1, 2);
        auto stream = [&](int c) {
            if (c < 3) at_stage(kv, lane16, lds0, c + 1, fresh(wv));
            else load_steps<5>(ws + WS_WO + (fresh(bi) & 7) * 102400, lds0 + HS_, 0, half * 12 + qt, 4, 12 + half * 8, lane16);
        };
        auto o_off = [&](int hd, unsigned& o16, unsigned& o8) {
            const unsigned oo = (qb * 16 + qt * 4 + 2 * half + (r32 >> 4)) * 20480;
            o16 = oo + (2 * hd + h) * 256 + r * 16; o8 = oo + 16384 + hd * 128 + r * 8;
        };
        attn_pass<false>(lds, ka, kb, va, qa1, qa2, qb1, qb2, 0.f, 0.f, oa, ob, [&](int c, auto tt) {
            constexpr int t = decltype(tt)::value;
            if constexpr (t == 0 || t == 4) {
                if (half == t / 4) {
                    stream(c);
                    if (c < 2) {
                        const Geo G(fresh(b), wv, lane);
                        const unsigned M = fresh(MARK);
                        const u32x4 mk = {M, M, M, M};
                        if (c == 0 || t == 0) {
                            const unsigned o = (c ? WS_HS : t ? WS_X1 : WS_H) + G.mt * 20480;
                            st16d(R, mk, G.foff, o); st16d(R, mk, G.foff + 1024, o); st8d(R, (u32x2){M, M}, G.hoff, o);
                            if (c == 0) { unsigned o16, o8; o_off(fresh(head), o16, o8); st16d(R, mk, o16, WS_O); if (!h) st8d(R, (u32x2){M, M}, o8, WS_O); }
                            else if (lane < 32) st8d(R, (u32x2){M, M}, g * (S_ * 64) + r * 8, WS_STH + G.mt * 1024 + G.nb * 128);
                        } else {
#pragma unroll
                            for (int i = 0; i < 6; ++i) st16d(R, mk, lane16 + (i % 3) * 1024, WS_G + ((G.mt3 + i / 3) * 48 + G.nb3 * 3) * 1024);
                        }
                    }
                }
            }
            if constexpr (t == 7) {
                vmwait<0>(); lgbar();
                STAMP_AT(1, 25 + c);
                if (c == 1 && tid == 0) set_flag(ws, FL_RT + b * 4);
            }
            if constexpr (t == 2) {
                if ((jobs >> c & 1) == 0) {
                } else if (c < 2) {
                    const int unit = wv * 256 + fresh(b);
                    const int mat = unit < 480 ? (unit * 410) >> 16 : unit < 800 ? 3 : 4, rt_nt = mat == 3 ? 6 : 5;
                    const int ru = unit - (mat < 4 ? mat * 160 : 800), rt_nb = mat < 4 ? (ru * 3277) >> 16 : (ru * 1366) >> 16, rt_ks = ru - rt_nb * (mat < 4 ? 20 : 48);
                    if (c == 0) {
                    const int pitch = mat == 3 ? 3072 : 1280, rr = (lane * (mat == 3 ? 43 : 52)) >> 9;
                    const unsigned vo = (rr * (mat == 3 ? 3072 - 192 : 1280 - 160) + lane16) & ((rr - 4) >> 31);
                    const bool late = mat == 0 && rt_ks >= 16;
                    const int d1 = (mat ? 4 : late ? 20 : 8) * pitch, d2 = (mat ? 8 : late ? 40 : 4) * pitch, d4 = (mat ? 16 : late ? 80 : 20) * pitch;
                    typedef const char* cptr;
                    const char* src = ((const __attribute__((address_space(4))) cptr*)__builtin_amdgcn_kernarg_segment_ptr())[1 + mat]
                                    + rfl(rt_nb * rt_nt * 32 + (rt_ks * (mat ? 32 : late ? 160 : 40) - (late ? 2544 : 0)) * pitch);
                    sfor<0, 8>([&](auto ii) __attribute__((always_inline)) {
                        constexpr int i = decltype(ii)::value;
                        glds_at<AT_RT + i * 1024>(src + ((i & 1) * d1 + ((i >> 1) & 1) * d2 + (i >> 2) * d4), vo, lds0 + wv * 8192);
                    });
                    } else {
                    const unsigned rt_dst = WS_WO + (mat < 4 ? mat * (D_ * D_ * 2) : WS_W4 - WS_WO) + (rt_nb * (mat == 4 ? 48 : 20) + rt_ks) * rt_nt * 1024 + lane16;
                    const unsigned rt_cs = WS_CS + ((mat == 3 ? 8 + rt_nb : rt_nb) * 20 + rt_ks) * 384 + r * 64 + g * 16;
                    const LAS char* rt_st = lds + AT_RT + wv * 8192 + g * 2048 + (r >> 2) * rt_nt * 32;
                    const LAS char* rt_s0 = rt_st + ((mat != 3 && (rt_nb & 1)) ? 32 : 0) + 16 * (lane & 3);
                    const LAS char* rt_s4 = mat == 3 ? rt_s0 + 128 : rt_st + ((rt_nb & 1) ? 0 : 128) + 8 * (lane & 3);
                    bf16x8 f[6];
#pragma unroll
                    for (int u = 0; u < 4; ++u) f[u] = cat8(ldstr(rt_s0 + (u >> 1) * 64 + (u & 1) * 8), ldstr(rt_s0 + (u >> 1) * 64 + (u & 1) * 8 + 1024));
                    f[4] = cat8(ldstr(rt_s4), ldstr(rt_s4 + 1024)); f[5] = cat8(ldstr(rt_s4 + 8), ldstr(rt_s4 + 1032));
#pragma unroll
                    for (int u = 0; u < 5; ++u) st16d(R, __builtin_bit_cast(u32x4, f[u]), rt_dst, u * 1024);
                    if (mat & 1) {
                        if (mat == 3) st16d(R, __builtin_bit_cast(u32x4, f[5]), rt_dst, 5120);
                        const bf16x8 ones = {0x3F80, 0x3F80, 0x3F80, 0x3F80, 0x3F80, 0x3F80, 0x3F80, 0x3F80};
                        f32x4 cs[6];
#pragma unroll
                        for (int u = 0; u < 6; ++u) cs[u] = MFMA16(f[u], ones, (f32x4){});
                        if (r < 6) st16d(R, __builtin_bit_cast(u32x4, r == 0 ? cs[0] : r == 1 ? cs[1] : r == 2 ? cs[2] : r == 3 ? cs[3] : r == 4 ? cs[4] : cs[5]), rt_cs, 0);
                    }
                    }
                } else {
                    for (unsigned spins = 0; spins < SPINS; ++spins) {
                        reread();
                        const u32x4 f = ld16d(R, lane16, FL_RT);
                        if (__all((f[0] & f[1] & f[2] & f[3]) == 1u)) break;
                        __builtin_amdgcn_s_sleep(1);
                    }
                }
            }
        });
        STAMP_AT(1, 3);
        bool done = false;
        for (int attempt = 0;; ++attempt) {
            LAS char* cbuf = lds + qt * 6144 + lane16;
#pragma unroll
            for (int i = 0; i < 3; ++i)
                *(LAS f32x4*)(cbuf + half * 3072 + i * 1024) = half ? (f32x4){oa[4 * i], oa[4 * i + 1], oa[4 * i + 2], oa[4 * i + 3]} : (f32x4){ob[4 * i], ob[4 * i + 1], ob[4 * i + 2], ob[4 * i + 3]};
            lgbar();
            {
                float o[12];
#pragma unroll
                for (int i = 0; i < 3; ++i) {
                    const f32x4 p = *(const LAS f32x4*)(cbuf + (half ^ 1) * 3072 + i * 1024);
#pragma unroll
                    for (int j = 0; j < 4; ++j) o[4 * i + j] = (half ? ob[4 * i + j] : oa[4 * i + j]) + p[j];
                }
                auto lr = __builtin_amdgcn_permlane32_swap(__float_as_uint(o[8]), __float_as_uint(o[8]), false, false);
                const float l = __uint_as_float(lr[1]);
                const bool ok = l >= 0x1p-60f && l <= 0x1p60f;
                if (!done) {
                    if (attempt == 1 || __all(ok)) {
                        const float inv = __builtin_amdgcn_rcpf(l);
                        unsigned oo16, oo8; o_off(head, oo16, oo8);
                        st16d(R, (u32x4){pk2(o[0] * inv, o[1] * inv), pk2(o[2] * inv, o[3] * inv), pk2(o[4] * inv, o[5] * inv), pk2(o[6] * inv, o[7] * inv)}, oo16, WS_O);
                        if (!h) st8d(R, (u32x2){pk2(o[8] * inv, o[9] * inv), pk2(o[10] * inv, o[11] * inv)}, oo8, WS_O);
                        done = true;
                    } else if (lane == 0) *(LAS unsigned*)(lds + L_BAD) = 1u;
                }
            }
            lgbar();
            if (attempt == 1 || rfl(*(const LAS unsigned*)(lds + L_BAD)) == 0) break;
            float ma = -INFINITY, mb = -INFINITY;
            at_stage(kv, lane16, lds0, 0, wv);
#pragma unroll 1
            for (int c = 0; c < 4; ++c) {
                vmwait<0>(); lgbar();
                if (c < 3) at_stage(kv, lane16, lds0, c + 1, wv);
                const LAS char* sl = lds + ((c & 1) ^ 1) * AT_SLOT;
#pragma unroll 1
                for (int t = 0; t < 8; ++t) {
                    const bf16x8 k1 = lds16(sl + ka + t * KVT); const bf16x4 k2 = lds8(sl + kb + t * KVT);
                    f32x16 s = MFMA32(k1, qa1, (f32x16){}), u = MFMA32(k1, qb1, (f32x16){});
                    s = MFMA32K8(k2, qa2, s); u = MFMA32K8(k2, qb2, u);
#pragma unroll
                    for (int r = 0; r < 16; ++r) { ma = fmaxf(ma, s[r]); mb = fmaxf(mb, u[r]); }
                }
            }
            ma = xmax32(ma); mb = xmax32(mb);
            LAS float* mbuf = (LAS float*)(lds + AT_SLOT) + qt * 128 + r32;
            if (!h) { mbuf[half * 64] = ma; mbuf[half * 64 + 32] = mb; }
            lgbar();
            ma = fmaxf(mbuf[0], mbuf[64]); mb = fmaxf(mbuf[32], mbuf[96]);
            lgbar();
            at_stage(kv, lane16, lds0, 0, wv);
            vmwait<0>(); lgbar();
            oa = (f32x16){}; ob = (f32x16){};
            attn_pass<true>(lds, ka, kb, va, qa1, qa2, qb1, qb2, ma, mb, oa, ob, [&](int c, auto tt) {
                constexpr int t = decltype(tt)::value;
                if constexpr (t == 0 || t == 4) { if (half == t / 4) stream(c); }
                if constexpr (t == 7) { vmwait<0>(); lgbar(); }
            });
        }
    }
    STAMP_AT(1, 4);
    const Geo G(fresh(b), wv, lane);
    const int nb = G.nb, nb3 = G.nb3, mt = G.mt, mt3 = G.mt3, gb = G.gb, hb = G.hb, r = lane & 15, g = lane >> 4;
    const unsigned foff = G.foff, hoff = G.hoff;
    if (wv >= 4) {
        const int lw = wv - 4;
        if (lane < 48) {
            const unsigned src = (lane < 24 ? nb * 7680 + lane * 16 : (8 + nb3) * 7680 + (lane - 24) * 16) + lw * 1920;
            f32x4 s = {};
#pragma unroll
            for (int k = 0; k < 5; ++k) s += __builtin_bit_cast(f32x4, ld16d(R, src + k * 384, WS_CS));
            *(LAS f32x4*)(lds + L_CP + (lw * 48 + lane) * 16) = s;
        }
        const char* w3 = ws + WS_W3 + nb3 * 122880;
        const char* w4 = ws + WS_W4 + nb * 245760;
#pragma unroll 1
        for (int st = 0; st < 3; ++st) {
            if (st == 1) put_stats(lds, R, WS_STH + mt * 1024, lw * 16, lane);
            vmwait<0>(); lgbar();
            const char* w = ws + (st == 0 ? WS_W1 : WS_W2) + nb * 102400;
            if (st < 2) { load_steps<5>(w, lds0 + st * 2 * HS_, 0, lw, 4, 10, lane16); load_steps<5>(w, lds0 + (1 + st) * HS_ % (2 * HS_), 10, lw ^ 2, 4, 6, lane16); }
            else load_steps<6>(w3, lds0 + HS_, 0, lw, 4, 14, lane16);
            if (st == 0 && lw == 3 && lane < 48) {
                const LAS f32x4* cp = (const LAS f32x4*)(lds + L_CP) + lane;
                *(LAS f32x4*)(lds + L_C1 + lane * 16) = (cp[0] + cp[48]) + (cp[96] + cp[144]);
            }
            vmwait<5>(); lgbar();
            if (st < 2) load_steps<5>(w, lds0 + (1 + st) * HS_ % (2 * HS_) + 6 * 5120, 16, lw, 4, 4, lane16);
            else { load_steps<6>(w3, lds0 + HS_ + 14 * 6144, 14, lw, 4, 4, lane16); load_steps<6>(w3, lds0, 18, lw, 4, 2, lane16); }
        }
        put_stats(lds, R, WS_STX + mt3 * 1024, lw * 32, lane); put_stats(lds, R, WS_STX + (mt3 + 1) * 1024, lw * 32 + 16, lane);
        vmwait<0>(); lgbar();
        load_steps<5>(w4, lds0 + 3 * 5120, 0, lw, 4, 19, lane16);
        lgbar();
        load_steps<5>(w4, lds0 + 22 * 5120, 19, lw, 4, 10, lane16); load_steps<5>(w4, lds0, 29, lw ^ 2, 4, 3, lane16);
        vmwait<15>(); lgbar();
        vmwait<0>(); lgbar();
        load_steps<5>(w4, lds0 + 3 * 5120, 32, lw, 4, 12, lane16);
        vmwait<0>(); lgbar();
        load_steps<5>(w4, lds0 + 15 * 5120, 44, lw, 4, 4, lane16);
        vmwait<0>(); lgbar();
        return;
    }
    f32x4 keep[5];
    {
        const char* xb = A.x + (size_t)(mt * 16 + r) * 1280;
        const u32x4 u = *(const u32x4*)(xb + gb * 2 + g * 16), v = *(const u32x4*)(xb + gb * 2 + 64 + g * 16); const u32x2 w = *(const u32x2*)(xb + hb * 2 + g * 8);
        keep[0] = (f32x4){blo(u[0]), bhi(u[0]), blo(u[1]), bhi(u[1])}; keep[1] = (f32x4){blo(u[2]), bhi(u[2]), blo(u[3]), bhi(u[3])};
        keep[2] = (f32x4){blo(v[0]), bhi(v[0]), blo(v[1]), bhi(v[1])}; keep[3] = (f32x4){blo(v[2]), bhi(v[2]), blo(v[3]), bhi(v[3])};
        keep[4] = (f32x4){blo(w[0]), bhi(w[0]), blo(w[1]), bhi(w[1])};
    }
    const int srow = mt * 16 + r + ((nb & 2) ? -(1 + (nb >> 2)) : 1 + (nb >> 2));
    const bool sin = (unsigned)srow < (unsigned)S_;
    const unsigned sro = WS_HS + ((srow & (S_ - 1)) >> 4) * 20480 + ((srow & 15) - r) * 16;
    float ia = 1.f, ib = 0.f;
#define FIRST(LD, N) _Pragma("unroll 1") for (unsigned spins = 0; spins < SPINS; ++spins) { reread(); \
            _Pragma("unroll") for (int k = 0; k < 8; ++k) { LD(k); SBAR(); } \
            unsigned m = 0; _Pragma("unroll") for (int k = 0; k < 4; ++k) m = N(m, k); \
            if (!marked(m)) break; } \
        _Pragma("unroll") for (int k = 8; k < PRE; ++k) { LD(k); SBAR(); }
#define AGAIN(LD, N, K0) if (marked(run)) { _Pragma("unroll 1") for (unsigned spins = 0; spins < SPINS; ++spins) { reread(); \
            _Pragma("unroll") for (int k_ = (K0); k_ < (K0) + 4; ++k_) LD(k_); \
            unsigned m = 0; _Pragma("unroll") for (int k_ = (K0); k_ < (K0) + 4; ++k_) m = N(m, k_); \
            if (!marked(m)) break; } }
#pragma unroll 1
    for (int st = 0; st < 3; ++st) {
        constexpr int PRE = 12;
        const unsigned ab = (st == 0 ? WS_O : st == 1 ? WS_H : WS_HS) + mt * 20480;
        const unsigned vlo = st == 0 ? g * 256 + r * 8 : lane16, vhi = vlo + (st == 0 ? 128 : 8);
        const LAS char* b0 = opaque(lds + (st == 0 ? HS_ : st == 1 ? 0 : 2 * HS_) + lane16);
        const LAS char* b1 = opaque(lds + (st == 0 ? 2 * HS_ : st == 1 ? HS_ : 0) + lane16);
        u32x4 a[20];
        bf16x8 bw[4][5];
        f32x4 acc[5];
#pragma unroll
        for (int t = 0; t < 5; ++t) { acc[t] = keep[t] * ia + ib; PIN(acc[t]); }
#pragma unroll
        for (int k = 0; k < 3; ++k)
#pragma unroll
            for (int t = 0; t < 5; ++t) { bw[k][t] = lds16(b0 + (k * 5 + t) * 1024); SBAR(); }
#define ALD(k) ld16d(R, lane16 + ((k) & 3) * 1024, ab + ((k) >> 2) * 4096)
#define LD5(k) do { if ((k) < 16) a[k] = ALD(k); else { const u32x2 lo_ = ld8d(R, vlo + ((k) & 3) * 1024, ab + 16384), hi_ = ld8d(R, vhi + ((k) & 3) * 1024, ab + 16384); \
            a[k] = (u32x4){lo_[0], lo_[1], hi_[0], hi_[1]}; } } while (0)
#define MK5(m, k) top4(m, a[k])
        FIRST(LD5, MK5)
        STAMP_AT(1, 5 + 4 * st);
        unsigned run = 0;
        sfor<0, 20>([&](auto kk) __attribute__((always_inline)) {
            constexpr int k = decltype(kk)::value;
            if (k == 7) lgbar();
            constexpr int hi = cmin(k + PRE, 20), lo = cmin(k + 5, 20);
            wait2<hi - lo + cmax(hi, 16) - cmax(lo, 16), k < 18 ? 10 : k == 18 ? 5 : 0>();
            SBAR();
#pragma unroll
            for (int t = 0; t < 5; ++t) {
                mma16(acc[t], bw[k & 3][t], a[k]);
                if (k + 3 < 20) bw[(k + 3) & 3][t] = lds16((k + 3 < 10 ? b0 + (k + 3) * 5120 : b1 + (k - 7) * 5120) + t * 1024);
                if constexpr (k + 4 < 20) { if (t == 0) { run = MK5((k & 3) ? run : 0u, k + 4); PIN(run); } }
                if constexpr (k + PRE < 20) { if (t == 1) LD5(k + PRE); }
                SBAR();
            }
            if constexpr ((k & 3) == 3 && k + 1 < 20) AGAIN(LD5, MK5, k + 1)
        });
        const f32x2 mv = *(const LAS f32x2*)(lds + L_ST + (wv * 16 + r) * 8);
        f32x4 c[5];
#pragma unroll
        for (int t = 0; t < 5; ++t) c[t] = *(const LAS f32x4*)(lds + L_C1 + t * 64 + g * 16);
        SBAR();
        MMA_DONE5(acc);
        STAMP_AT(1, 6 + 4 * st);
        unsigned pk[10];
        if (st == 1) {
            ia = __builtin_amdgcn_rsqf(mv[1] + EPS); ib = -mv[0] * ia;
            const Gelu gelu(ia * __builtin_amdgcn_rsqf(mv[1] * ia * ia + EPS));
#pragma unroll
            for (int j = 0; j < 10; ++j) {
                const f32x2 u = {acc[j >> 1][(j & 1) * 2], acc[j >> 1][(j & 1) * 2 + 1]}, cc = {c[j >> 1][(j & 1) * 2], c[j >> 1][(j & 1) * 2 + 1]};
                const unsigned y = gelu(u - cc * mv[0]);
                pk[j] = sin ? y : 0u;
            }
        } else {
#pragma unroll
            for (int j = 0; j < 10; ++j) pk[j] = pk2(acc[j >> 1][(j & 1) * 2], acc[j >> 1][(j & 1) * 2 + 1]);
        }
        const unsigned ob = st == 1 ? sro : (st == 0 ? WS_H : WS_X1) + mt * 20480;
        st16d(R, (u32x4){pk[0], pk[1], pk[2], pk[3]}, foff, ob);
        st16d(R, (u32x4){pk[4], pk[5], pk[6], pk[7]}, foff + 1024, ob);
        st8d(R, (u32x2){pk[8], pk[9]}, hoff + ob, 0);
        if (st != 1) {
            f32x4 s1 = acc[0], s2 = acc[0] * acc[0];
#pragma unroll
            for (int t = 1; t < 5; ++t) { s1 += acc[t]; s2 = __builtin_elementwise_fma(acc[t], acc[t], s2); }
            const float p1 = xsum32(xsum16((s1[0] + s1[1]) + (s1[2] + s1[3]))), p2 = xsum32(xsum16((s2[0] + s2[1]) + (s2[2] + s2[3])));
            if (g == 0) st8d(R, (u32x2){__float_as_uint(p1), __float_as_uint(p2)}, r * 8, (st == 0 ? WS_STH : WS_STX) + mt * 1024 + nb * 128);
            const float ka_ = st == 0 ? 0.f : 1.f;
#pragma unroll
            for (int t = 0; t < 5; ++t) keep[t] = keep[t] * ka_ + acc[t];
            ia = 0.f;
        }
        STAMP_AT(1, 7 + 4 * st);
        lgbar();
        STAMP_AT(1, 8 + 4 * st);
    }
    {
        constexpr int RA = 9, PRE = 8;
        const unsigned ab = WS_X1 + mt3 * 20480;
        const LAS char* b0 = opaque(lds + HS_ + lane16);
        const LAS char* b1 = opaque(lds + HS_ + 61440 + lane16);
        const LAS char* b2 = opaque(lds + lane16);
        u32x4 a[RA][2];
        bf16x8 bw[3][6];
        f32x4 acc[2][6];
#pragma unroll
        for (int k = 0; k < 2; ++k)
#pragma unroll
            for (int t = 0; t < 6; ++t) { bw[k][t] = lds16(b0 + (k * 6 + t) * 1024); SBAR(); }
#define LD3(k) do { a[(k) % RA][0] = ALD(k); a[(k) % RA][1] = ALD((k) + 20); } while (0)
#define MK3(m, k) top4(top4(m, a[(k) % RA][0]), a[(k) % RA][1])
        FIRST(LD3, MK3)
        STAMP_AT(1, 17);
        unsigned run = 0;
        sfor<0, 20>([&](auto kk) __attribute__((always_inline)) {
            constexpr int k = decltype(kk)::value;
            if (k == 8) lgbar();
            wait2<2 * (cmin(k + PRE, 20) - cmin(k + 5, 20)), k < 19 ? 6 : 0>();
            SBAR();
#pragma unroll
            for (int t = 0; t < 6; ++t) {
                if (k == 0) { mma16z(acc[0][t], bw[0][t], a[0][0]); mma16z(acc[1][t], bw[0][t], a[0][1]); }
                else { mma16(acc[0][t], bw[k % 3][t], a[k % RA][0]); mma16(acc[1][t], bw[k % 3][t], a[k % RA][1]); }
                if (k + 2 < 20) bw[(k + 2) % 3][t] = lds16((k + 2 < 10 ? b0 + (k + 2) * 6144 : k + 2 < 18 ? b1 + (k - 8) * 6144 : b2 + (k - 16) * 6144) + t * 1024);
                if constexpr (k + 4 < 20) { if (t == 0) { run = MK3((k & 3) ? run : 0u, k + 4); PIN(run); } }
                if constexpr (k + PRE < 20) { if (t == 1) LD3(k + PRE); }
                SBAR();
            }
            if constexpr ((k & 3) == 3 && k + 1 < 20) AGAIN(LD3, MK3, k + 1)
        });
        f32x4 c3[6];
#pragma unroll
        for (int t = 0; t < 6; ++t) c3[t] = *(const LAS f32x4*)(lds + L_C3 + t * 64 + g * 16);
        const f32x2 mv0 = *(const LAS f32x2*)(lds + L_ST + (wv * 32 + r) * 8), mv1 = *(const LAS f32x2*)(lds + L_ST + (wv * 32 + 16 + r) * 8);
        lgbar();
        MMA_DONE6(acc[0]); MMA_DONE6(acc[1]);
        STAMP_AT(1, 18);
#pragma unroll
        for (int mi = 0; mi < 2; ++mi) {
            const float m3 = mi ? mv1[0] : mv0[0];
            const Gelu gelu(__builtin_amdgcn_rsqf((mi ? mv1[1] : mv0[1]) + EPS));
#pragma unroll
            for (int p = 0; p < 3; ++p) {
                u32x4 o;
#pragma unroll
                for (int j = 0; j < 4; ++j) {
                    const int t = 2 * p + (j >> 1), e = (j & 1) * 2;
                    o[j] = gelu((f32x2){acc[mi][t][e], acc[mi][t][e + 1]} - (f32x2){c3[t][e], c3[t][e + 1]} * m3);
                }
                st16d(R, o, lane16, WS_G + ((mt3 + mi) * 48 + nb3 * 3 + p) * 1024);
            }
        }
        STAMP_AT(1, 19);
        lgbar();
        STAMP_AT(1, 20);
    }
    {
        constexpr int RA = 13, PRE = 12;
        const unsigned ab = WS_G + mt * 49152;
        const LAS char* bp[3] = {opaque(lds + lane16), opaque(lds + 12 * 5120 + lane16), opaque(lds + 24 * 5120 + lane16)};
#define BLD(k, t) lds16(bp[(((k) + 3) & 31) / 12] + ((((k) + 3) & 31) % 12) * 5120 + (t) * 1024)
        u32x4 a[RA];
        bf16x8 bw[4][5];
#pragma unroll
        for (int t = 0; t < 5; ++t) PIN(keep[t]);
#pragma unroll
        for (int k = 0; k < 3; ++k)
#pragma unroll
            for (int t = 0; t < 5; ++t) { bw[k][t] = BLD(k, t); SBAR(); }
#define LD4(k) a[(k) % RA] = ALD(k)
#define MK4(m, k) top4(m, a[(k) % RA])
        FIRST(LD4, MK4)
        STAMP_AT(1, 21);
        unsigned run = 0;
        sfor<0, 48>([&](auto kk) __attribute__((always_inline)) {
            constexpr int k = decltype(kk)::value;
            if (k == 9 || k == 29 || k == 41) lgbar();
            wait2<cmin(k + PRE, 48) - cmin(k + 5, 48), k < 46 ? 10 : k == 46 ? 5 : 0>();
            SBAR();
#pragma unroll
            for (int t = 0; t < 5; ++t) {
                mma16(keep[t], bw[k & 3][t], a[k % RA]);
                if (k + 3 < 48) bw[(k + 3) & 3][t] = BLD(k + 3, t);
                if constexpr (k + 4 < 48) { if (t == 0) { run = MK4((k & 3) ? run : 0u, k + 4); PIN(run); } }
                if constexpr (k + PRE < 48) { if (t == 1) LD4(k + PRE); }
                SBAR();
            }
            if constexpr ((k & 3) == 3 && k + 1 < 48) AGAIN(LD4, MK4, k + 1)
        });
        MMA_DONE5(keep);
        STAMP_AT(1, 22);
        unsigned pk[10];
#pragma unroll
        for (int j = 0; j < 10; ++j) pk[j] = pk2(keep[j >> 1][(j & 1) * 2], keep[j >> 1][(j & 1) * 2 + 1]);
        char* ob = A.out + (size_t)(mt * 16 + r) * 1280;
        *(u32x4*)(ob + gb * 2 + g * 16) = (u32x4){pk[0], pk[1], pk[2], pk[3]};
        *(u32x4*)(ob + gb * 2 + 64 + g * 16) = (u32x4){pk[4], pk[5], pk[6], pk[7]};
        *(u32x2*)(ob + hb * 2 + g * 8) = (u32x2){pk[8], pk[9]};
    }
}

extern "C" void kernel_launch(void* const* d_in, const int* in_sizes, int n_in, void* d_out, int out_size, void* d_ws, size_t ws_size, hipStream_t stream) {
    Args a;
    a.x = (const char*)d_in[0]; a.wo = (const char*)d_in[2]; a.w1 = (const char*)d_in[4]; a.w2 = (const char*)d_in[5];
    a.w3 = (const char*)d_in[6]; a.w4 = (const char*)d_in[7]; a.ws = (char*)d_ws; a.out = (char*)d_out;
    (void)hipFuncSetAttribute((const void*)k_qkv, hipFuncAttributeMaxDynamicSharedMemorySize, 163840);
    (void)hipFuncSetAttribute((const void*)k_main, hipFuncAttributeMaxDynamicSharedMemorySize, 163840);
    k_qkv<<<240, 512, 163840, stream>>>(a.x, (const char*)d_in[1], a.ws);
    k_main<<<256, 512, 163840, stream>>>(a);
}
```
